```python
import torch

def _softmax(x, axis=-1):
    return torch.softmax(x, dim=axis)
def _gelu(x):
    return torch.nn.functional.gelu(x, approximate="tanh")
def _silu(x):
    return torch.nn.functional.silu(x)
def _sigmoid(x):
    return torch.sigmoid(x)
def _layernorm(x, axis=-1, eps=1e-5):
    m = x.mean(dim=axis, keepdim=True)
    v = x.var(dim=axis, keepdim=True, unbiased=False)
    return (x - m) / torch.sqrt(v + eps)
def _scatter_add(tgt, idx, src, axis=0):
    return tgt.clone().index_add_(axis, idx, src)
def _segment_sum(src, seg, n):
    out = torch.zeros((n,) + src.shape[1:], dtype=src.dtype)
    return out.index_add_(0, seg, src)
def _pad(x, pw):
    flat = tuple(v for pair in reversed(pw) for v in pair)
    return torch.nn.functional.pad(x, flat)

kernel_name = 'standalone__gqa_block_cached__near_ridge'

def setup_inputs():

    _w = (lambda a: (lambda t: t.to(torch.bfloat16) if t.is_floating_point() else t)(torch.as_tensor(a)))
    B, Sq, D = 8, 160, 448
    Hq, Hkv, Sk, Dh = 16, 8, 1024, 256
    assert Hq % Hkv == 0, (Hq, Hkv)
    return {
        "x": _w((lambda *s: torch.randn(*s, dtype=torch.bfloat16) * 0.8)(B, Sq, D)),
        "k_cache": _w((lambda *s: torch.randn(*s, dtype=torch.bfloat16) * 0.8)(B, Hkv, Sk, Dh)),
        "v_cache": _w((lambda *s: torch.randn(*s, dtype=torch.bfloat16) * 0.05)(B, Hkv, Sk, Dh)),
        "wq": _w((lambda *s: torch.randn(*s, dtype=torch.bfloat16) * 0.8)(D, Hq * Dh) * (D ** -0.5)),
        "wo": _w((lambda *s: torch.randn(*s, dtype=torch.bfloat16) * 0.05)(Hq * Dh, D) * ((Hq * Dh) ** -0.5)),
    }

def reference(x, k_cache, v_cache, wq, wo):
    B, Sq, D = x.shape
    Hq, Dh = 16, 256
    Hkv = k_cache.shape[1]
    rep = Hq // Hkv
    q = (lambda x, *d: x.permute(*d))((x @ wq).reshape(B, Sq, Hq, Dh), 0, 2, 1, 3)
    k = (lambda x, n, axis: torch.repeat_interleave(x, n, dim=axis))(k_cache, rep, axis=1)
    v = (lambda x, n, axis: torch.repeat_interleave(x, n, dim=axis))(v_cache, rep, axis=1)
    scores = torch.einsum("bhqd,bhkd->bhqk", q, k) * (Dh ** -0.5)
    p = _softmax(scores, axis=-1)
    o = torch.einsum("bhqk,bhkd->bhqd", p, v)
    return (lambda x, *d: x.permute(*d))(o, 0, 2, 1, 3).reshape(B, Sq, Hq * Dh) @ wo
```

```cpp
#include <hip/hip_runtime.h>
#include <hip/hip_bf16.h>
typedef unsigned short u16;
__device__ inline float bf2f(u16 v){ return __uint_as_float(((unsigned)v)<<16); }
__device__ inline u16 f2bf(float f){ __hip_bfloat16 b = __float2bfloat16(f); return *(u16*)&b; }
constexpr int B=8,SQ=160,D=448,HQ=16,HKV=8,SK=1024,DH=256;
__global__ void qproj(const u16* x,const u16* wq,u16* q){
  int row=blockIdx.x;
  for(int n=threadIdx.x;n<HQ*DH;n+=blockDim.x){ float a=0; for(int c=0;c<D;c++) a+=bf2f(x[row*D+c])*bf2f(wq[c*HQ*DH+n]); q[row*HQ*DH+n]=f2bf(a);} }
__global__ void attn(const u16* q,const u16* k,const u16* v,u16* o){
  int row=blockIdx.x, h=blockIdx.y, b=row/SQ; int kv=h/2; __shared__ float s[SK]; __shared__ float qs[DH]; __shared__ float red[256];
  int t=threadIdx.x; qs[t]=bf2f(q[row*HQ*DH+h*DH+t]); __syncthreads();
  const u16* K=k+((size_t)(b*HKV+kv))*SK*DH; const u16* V=v+((size_t)(b*HKV+kv))*SK*DH;
  float m=-1e30f; for(int j=t;j<SK;j+=256){ float a=0; for(int d=0;d<DH;d++) a+=qs[d]*bf2f(K[j*DH+d]); a*=0.0625f; s[j]=a; m=fmaxf(m,a);}
  red[t]=m; __syncthreads(); for(int o2=128;o2>0;o2>>=1){ if(t<o2) red[t]=fmaxf(red[t],red[t+o2]); __syncthreads(); } m=red[0]; __syncthreads();
  float l=0; for(int j=t;j<SK;j+=256){ float p=__expf(s[j]-m); s[j]=p; l+=p;} red[t]=l; __syncthreads(); for(int o2=128;o2>0;o2>>=1){ if(t<o2) red[t]+=red[t+o2]; __syncthreads(); } l=red[0];
  float a=0; for(int j=0;j<SK;j++) a+=s[j]*bf2f(V[j*DH+t]); o[row*HQ*DH+h*DH+t]=f2bf(a/l); }
__global__ void oproj(const u16* o,const u16* wo,u16* out){ int row=blockIdx.x; for(int n=threadIdx.x;n<D;n+=blockDim.x){ float a=0; for(int c=0;c<HQ*DH;c++) a+=bf2f(o[row*HQ*DH+c])*bf2f(wo[c*D+n]); out[row*D+n]=f2bf(a);} }
extern "C" void kernel_launch(void* const* d_in, const int* in_sizes, int n_in, void* d_out, int out_size, void* d_ws, size_t ws_size, hipStream_t stream) {
  const u16* x=(const u16*)d_in[0]; const u16* k=(const u16*)d_in[1]; const u16* v=(const u16*)d_in[2]; const u16* wq=(const u16*)d_in[3]; const u16* wo=(const u16*)d_in[4];
  u16* q=(u16*)d_ws; u16* o=q+B*SQ*HQ*DH;
  qproj<<<B*SQ,256,0,stream>>>(x,wq,q);
  attn<<<dim3(B*SQ,HQ),256,0,stream>>>(q,k,v,o);
  oproj<<<B*SQ,256,0,stream>>>(o,wo,(u16*)d_out);
}
```

```cpp
#include <hip/hip_runtime.h>
#include <stdint.h>
#include <type_traits>

typedef unsigned int u32;
typedef unsigned short u16;
typedef __bf16 bf16x8 __attribute__((ext_vector_type(8)));
typedef __bf16 bf16x2 __attribute__((ext_vector_type(2)));
typedef short s16x4 __attribute__((ext_vector_type(4)));
typedef short s16x8 __attribute__((ext_vector_type(8)));
typedef float f32x4 __attribute__((ext_vector_type(4)));
typedef float f32x2 __attribute__((ext_vector_type(2)));
typedef u32 u32x2 __attribute__((ext_vector_type(2)));
typedef u32 u32x4 __attribute__((ext_vector_type(4)));
typedef u32x4 rsrc_t;
#define LAS __attribute__((address_space(3)))
#define DI __device__ __forceinline__

constexpr int NB = 8, SQ = 160, DM = 448, HQ = 16, HKV = 8, SK = 1024, DH = 256;
constexpr int ROWS = 80;
constexpr float SCL = 0.0625f * 1.4426950408889634f;
constexpr float THR = 20.0f;

constexpr u32 L_VF = 0;
constexpr u32 L_KC = 65536;
constexpr u32 L_P = 131072;
constexpr u32 L_MX = 151552;
constexpr u32 L_FLAG = 152832;
constexpr u32 L_LS = L_KC + 6 * 8192;
constexpr u32 L_QO = 122880;
constexpr u32 L_END = 163840;

DI void dma(rsrc_t r, u32 voff, u32 soff, u32 ldsaddr) {
  asm volatile("s_mov_b32 m0, %0\n\ts_nop 0\n\tbuffer_load_dwordx4 %1, %2, %3 offen lds" ::"s"(ldsaddr), "v"(voff), "s"(r), "s"(soff) : "memory");
}
DI void dma_dep(rsrc_t r, u32 voff, u32 soff, u32 ldsaddr, bf16x8 dep, bf16x8 dep2) {
  asm volatile("s_mov_b32 m0, %0\n\ts_nop 0\n\tbuffer_load_dwordx4 %1, %2, %3 offen lds" ::"s"(ldsaddr), "v"(voff), "s"(r), "s"(soff), "v"(dep), "v"(dep2) : "memory");
}
DI void pin4(const bf16x8 (&A)[4]) { asm volatile("" ::"v"(A[0]), "v"(A[1]), "v"(A[2]), "v"(A[3]) : "memory"); }
template <int N> DI void wait_vm() { asm volatile("s_waitcnt vmcnt(%0)" ::"i"(N) : "memory"); }
DI void bar() { asm volatile("s_barrier" ::: "memory"); }
DI void bar_lds() { asm volatile("s_waitcnt lgkmcnt(0)\n\ts_barrier" ::: "memory"); }
DI bf16x8 ld128(u32 a) { return *(const LAS bf16x8*)a; }
DI f32x4 ldf4(u32 a) { return *(const LAS f32x4*)a; }
DI bf16x8 ldtr(u32 a0, u32 a1) {
  s16x4 lo = __builtin_amdgcn_ds_read_tr16_b64_v4i16((LAS s16x4*)a0);
  s16x4 hi = __builtin_amdgcn_ds_read_tr16_b64_v4i16((LAS s16x4*)a1);
  s16x8 v = __builtin_shufflevector(lo, hi, 0, 1, 2, 3, 4, 5, 6, 7);
  return __builtin_bit_cast(bf16x8, v);
}
DI u32 pk(float lo, float hi) { f32x2 v = {lo, hi}; return __builtin_bit_cast(u32, __builtin_convertvector(v, bf16x2)); }
DI f32x4 mfma(bf16x8 a, bf16x8 b, f32x4 c) { return __builtin_amdgcn_mfma_f32_16x16x32_bf16(a, b, c, 0, 0, 0); }
DI rsrc_t mkrsrc(const void* p, u32 bytes) { const uint64_t a = (uint64_t)p; return rsrc_t{(u32)a, (u32)(a >> 32) & 0xffffu, bytes, 0x00020000u}; }
DI float xmax(float v) {
  auto a = __builtin_amdgcn_permlane16_swap(__float_as_uint(v), __float_as_uint(v), false, false);
  v = fmaxf(__uint_as_float(a[0]), __uint_as_float(a[1]));
  auto b = __builtin_amdgcn_permlane32_swap(__float_as_uint(v), __float_as_uint(v), false, false);
  return fmaxf(__uint_as_float(b[0]), __uint_as_float(b[1]));
}
DI float xsum(float v) {
  auto a = __builtin_amdgcn_permlane16_swap(__float_as_uint(v), __float_as_uint(v), false, false);
  v = __uint_as_float(a[0]) + __uint_as_float(a[1]);
  auto b = __builtin_amdgcn_permlane32_swap(__float_as_uint(v), __float_as_uint(v), false, false);
  return __uint_as_float(b[0]) + __uint_as_float(b[1]);
}


DI void readA(bf16x8 (&A)[4], const u32 (&ab)[4], int e) {
#pragma unroll
  for (int dt = 0; dt < 4; ++dt) A[dt] = ldtr(ab[dt] + 4096 * e, ab[dt ^ 2] + 4096 * e + 512);
}

template <int BS, int WA1, int WA0, bool REFILL, bool NEXT>
DI void gstep(f32x4 (&acc)[4][5], bf16x8 (&A0)[4], const bf16x8 (&B0)[5], const bf16x8 (&B1)[5], u32 (&ab)[4], u32& slot, int& dslot,
              rsrc_t rr, u32 rsoff, u32 rstride, u32 rvoff) {
  bf16x8 A1[4];
  wait_vm<WA1>();
  readA(A1, ab, 1);
#pragma unroll
  for (int dt = 0; dt < 4; ++dt) {
#pragma unroll
    for (int qt = 0; qt < 5; ++qt) acc[dt][qt] = mfma(A0[dt], B0[qt], acc[dt][qt]);
    if (REFILL) { if (dt == 0) pin4(A0); dma(rr, (dt & 1) ? (rvoff ^ 64u) : rvoff, rsoff + dt * rstride, slot + dt * 1024); }
    __builtin_amdgcn_sched_barrier(0);
  }
#pragma unroll
  for (int dt = 0; dt < 4; ++dt) {
#pragma unroll
    for (int qt = 0; qt < 5; ++qt) acc[dt][qt] = mfma(A1[dt], B1[qt], acc[dt][qt]);
    if (REFILL) { if (dt == 0) pin4(A1); dma(rr, (dt & 1) ? (rvoff ^ 64u) : rvoff, rsoff + (4 + dt) * rstride, slot + (4 + dt) * 1024); }
    if (NEXT && dt == 1) {
      wait_vm<WA0>();
#pragma unroll
      for (int i = 0; i < 4; ++i) ab[i] += dslot;
      readA(A0, ab, 0);
    }
    __builtin_amdgcn_sched_barrier(0);
  }
  slot += dslot; dslot = -dslot;
}
template <int BS> DI void readB(bf16x8 (&B0)[5], bf16x8 (&B1)[5], u32 bb0, u32 bb1) {
#pragma unroll
  for (int qt = 0; qt < 5; ++qt) B0[qt] = ld128(bb0 + qt * BS);
#pragma unroll
  for (int qt = 0; qt < 5; ++qt) B1[qt] = ld128(bb1 + qt * BS);
}
DI void writeB(const f32x4 (&acc)[4][5], const float (&sc)[5], u32 base, u32 lane) {
  const u32 q = lane & 15, g = lane >> 4;
  const u32 a = base + ((g >> 1) * 16 + q) * 16 + (g & 1) * 8;
#pragma unroll
  for (int dt = 0; dt < 4; ++dt)
#pragma unroll
    for (int qt = 0; qt < 5; ++qt) {
      u32x2 w = {pk(acc[dt][qt][0] * sc[qt], acc[dt][qt][1] * sc[qt]), pk(acc[dt][qt][2] * sc[qt], acc[dt][qt][3] * sc[qt])};
      *(LAS u32x2*)(a + ((dt >> 1) * 5 + qt) * 1024 + (dt & 1) * 512) = w;
    }
}

DI u32 kv_tile(u32 cu, u32 s) { const u32 d = (cu - s) & 3; const u32 g = d == 0 ? 0 : (d == 1 ? 2 : 3); return (s - g) & 15; }

__global__ __launch_bounds__(512, 2) void fused_attn(const u16* __restrict__ x, const u16* __restrict__ kc, const u16* __restrict__ vc,
                                                     const u16* __restrict__ wq, const u16* __restrict__ wo, u32x4* __restrict__ part) {
  extern __shared__ __attribute__((aligned(16))) char smem[];
  const u32 lane = threadIdx.x & 63, wid = __builtin_amdgcn_readfirstlane(threadIdx.x >> 6), w = wid & 3;
  const u32 blk = blockIdx.x, kvh = blk & 7, idx = blk >> 3, b = idx >> 2, cu = idx & 3, head = 2 * kvh + (cu >> 1), half = cu & 1;
  const u32 q = lane & 15, g = lane >> 4;
  const u32 lr = lane >> 3, lc = (lane & 7) ^ (lr & 6);
  const u32 rot = 2 * b + half;
  const rsrc_t rWo = mkrsrc(wo + (size_t)head * DH * DM, DH * DM * 2);
  const u32 voffWo = lr * (DM * 2) + lc * 16;

  f32x4 acc[4][5];
  bf16x8 A0[4];
  u32 ab[4], slot; int dslot;
  {
    const u32 q4 = (lane & 15) >> 2, p = lane & 3, f0 = (q4 & 2) ^ ((g & 1) << 2);
#pragma unroll
    for (int i = 0; i < 4; ++i) ab[i] = g * 1024 + q4 * 128 + 16 * (((2 * i) ^ f0) | (p >> 1)) + 8 * (p & 1);
  }
  u32 nb;

  if (wid < 4) {
    const rsrc_t rK = mkrsrc(kc + (size_t)(b * HKV + kvh) * SK * DH, SK * DH * 2);
    const u32 voffK = lr * 512 + lc * 16;
    const u32 ks0 = L_KC + 8192 * (w < 3 ? w : 6), ks1 = L_KC + 8192 * (w < 3 ? 3 + w : 7);
    {
      const rsrc_t rX = mkrsrc(x + (size_t)(b * SQ + half * ROWS) * DM, ROWS * DM * 2);
#pragma unroll
      for (int i = 0; i < 18; ++i) {
        u32 n = 4 * i + w; n = n < 70 ? n : 69;
        const u32 vp = n / 10, rg = n - 10 * vp; u32 v = vp + rot % 7; v = v >= 7 ? v - 7 : v;
        dma(rX, voffWo, rg * 8 * (DM * 2) + v * 128, L_END - 10240 * (vp + 1) + rg * 1024);
      }
    }
    wait_vm<13>(); bar();
    wait_vm<5>(); bar();
    wait_vm<0>(); bar();
    auto issueK = [&](u32 s, u32 ks) __attribute__((always_inline)) {
      const u32 so = (kv_tile(cu, s) * 64 + 16 * w) * 512;
#pragma unroll
      for (int v = 0; v < 4; ++v) { dma(rK, voffK, so + 128 * v, ks + v * 1024); dma(rK, voffK, so + 4096 + 128 * v, ks + 4096 + v * 1024); }
    };
    issueK(0, ks0);
    bar();
    if (w < 3) issueK(1, ks1);
    bf16x8 Q[8][5];
#pragma unroll
    for (int u = 0; u < 8; ++u)
#pragma unroll
      for (int qt = 0; qt < 5; ++qt) Q[u][qt] = ld128(L_QO + lane * 16 + (u * 5 + qt) * 1024);
    bar_lds();
    if (w == 3) issueK(1, ks1);
    __builtin_amdgcn_s_setprio(2);

    u32 kb = ks0 + (q >> 3) * 4096 + (q & 7) * 128 + 16 * (g ^ (q & 6));
    u32 kslot = ks0; int dk = (int)(ks1 - ks0);
    u32 pw = L_P + (((2 * w + (g >> 1)) & 3) * 16 + q) * 16 + (g & 1) * 8 + (w >> 1) * 5120; int dp = 10240;
    float mref[5], l[5];
#pragma unroll
    for (int qt = 0; qt < 5; ++qt) { mref[qt] = -__builtin_inff(); l[qt] = 0.f; }
    f32x4 S[5];
    bf16x8 K0, K1;
    wait_vm<14>();
    K0 = ld128(kb); K1 = ld128(kb ^ 64);

    auto softmax_write = [&](bool add) __attribute__((always_inline)) {
      float mx = -__builtin_inff();
      float ps[5];
#pragma unroll
      for (int qt = 0; qt < 5; ++qt) {
        float t0 = fmaf(S[qt][0], SCL, -mref[qt]), t1 = fmaf(S[qt][1], SCL, -mref[qt]), t2 = fmaf(S[qt][2], SCL, -mref[qt]), t3 = fmaf(S[qt][3], SCL, -mref[qt]);
        mx = fmaxf(fmaxf(mx, t0), t1); mx = fmaxf(fmaxf(mx, t2), t3);
        t0 = __builtin_amdgcn_exp2f(t0); t1 = __builtin_amdgcn_exp2f(t1); t2 = __builtin_amdgcn_exp2f(t2); t3 = __builtin_amdgcn_exp2f(t3);
        u32x2 wv = {pk(t0, t1), pk(t2, t3)};
        *(LAS u32x2*)(pw + qt * 1024) = wv;
        ps[qt] = (t0 + t1) + (t2 + t3);
      }
      const bool viol = __any(mx > THR);
      if (add && !viol) {
#pragma unroll
        for (int qt = 0; qt < 5; ++qt) l[qt] += ps[qt];
      }
      return viol;
    };
    auto fixup = [&](bool self, u32 par) __attribute__((always_inline)) {
#pragma unroll
      for (int qt = 0; qt < 5; ++qt) {
        float m = fmaxf(fmaxf(S[qt][0], S[qt][1]), fmaxf(S[qt][2], S[qt][3])) * SCL;
        m = xmax(m);
        *(LAS float*)(L_MX + (qt * 16 + q) * 16 + w * 4) = m;
      }
      bar_lds();
#pragma unroll
      for (int qt = 0; qt < 5; ++qt) {
        const f32x4 m4 = ldf4(L_MX + (qt * 16 + q) * 16);
        const float mn = fmaxf(fmaxf(fmaxf(m4[0], m4[1]), fmaxf(m4[2], m4[3])), mref[qt]);
        l[qt] *= __builtin_amdgcn_exp2f(mref[qt] - mn); mref[qt] = mn;
      }
      softmax_write(self);
      if (w == 0) *(LAS u32*)(L_FLAG + par * 4) = 0;
      bar_lds();
    };
    auto qk = [&](u32 s, auto mode) __attribute__((always_inline)) {
      constexpr int MODE = decltype(mode)::value;
      const u32 so = (kv_tile(cu, s + 2) * 64 + 16 * w) * 512;
#pragma unroll
      for (int v = 0; v < 4; ++v) {
        bf16x8 N0, N1;
        if (v < 3) {
          if (MODE == 0) wait_vm<12>(); else if (MODE == 1) { if (v == 0) wait_vm<12>(); else if (v == 1) wait_vm<10>(); else wait_vm<8>(); }
          else { if (v == 0) wait_vm<4>(); else if (v == 1) wait_vm<2>(); else wait_vm<0>(); }
          N0 = ld128(kb + (v + 1) * 1024); N1 = ld128((kb ^ 64) + (v + 1) * 1024);
        }
#pragma unroll
        for (int qt = 0; qt < 5; ++qt) S[qt] = mfma(K0, Q[2 * v][qt], v == 0 ? f32x4{0.f, 0.f, 0.f, 0.f} : S[qt]);
        if (MODE == 0) dma_dep(rK, voffK, so + 128 * v, kslot + v * 1024, K0, K1);
#pragma unroll
        for (int qt = 0; qt < 5; ++qt) S[qt] = mfma(K1, Q[2 * v + 1][qt], S[qt]);
        if (MODE == 0) dma(rK, voffK, so + 4096 + 128 * v, kslot + 4096 + v * 1024);
        if (v < 3) { K0 = N0; K1 = N1; }
        __builtin_amdgcn_sched_barrier(0);
      }
      kb += dk; kslot += dk; dk = -dk;
      if (MODE != 2) {
        if (MODE == 0) wait_vm<14>(); else wait_vm<6>();
        K0 = ld128(kb); K1 = ld128(kb ^ 64);
      }
    };
    using M0_ = std::integral_constant<int, 0>; using M1_ = std::integral_constant<int, 1>; using M2_ = std::integral_constant<int, 2>;
    auto finish = [&](u32 s) __attribute__((always_inline)) {
      const bool viol = softmax_write(true);
      if (viol) *(LAS u32*)(L_FLAG + (s & 1) * 4) = 1;
      bar_lds();
      const u32 fl = __builtin_amdgcn_readfirstlane(*(volatile LAS u32*)(L_FLAG + (s & 1) * 4));
      if (__builtin_expect(fl != 0, 0)) fixup(viol, s & 1);
      pw += dp; dp = -dp;
    };
    qk(0, M0_{});
    fixup(true, 0);
    pw += dp; dp = -dp;
    for (u32 s = 1; s < 14; ++s) { qk(s, M0_{}); finish(s); }
    qk(14, M1_{}); finish(14);
    qk(15, M2_{});
    nb = w;
    if (w < 3) {
#pragma unroll
      for (int j = 0; j < 2; ++j)
#pragma unroll
        for (int i = 0; i < 8; ++i)
          dma_dep(rWo, (i & 1) ? (voffWo ^ 64u) : voffWo, (((rot + j) & 3) * 64 + 8 * i) * (DM * 2) + nb * 128, (j ? ks1 : ks0) + i * 1024, K0, K1);
    }
    finish(15);
    __builtin_amdgcn_s_setprio(0);
#pragma unroll
    for (int qt = 0; qt < 5; ++qt) *(LAS float*)(L_LS + (qt * 16 + q) * 16 + w * 4) = xsum(l[qt]);
    bar_lds();
    slot = ks0; dslot = (int)(ks1 - ks0);
#pragma unroll
    for (int i = 0; i < 4; ++i) ab[i] += slot;
    if (w < 3) { wait_vm<12>(); readA(A0, ab, 0); }
    bar();
  } else {
    const rsrc_t rV = mkrsrc(vc + (size_t)(b * HKV + kvh) * SK * DH, SK * DH * 2);
    const rsrc_t rWq = mkrsrc(wq + head * DH, (DM * HQ * DH - head * DH) * 2);
    const u32 voffV = lr * 512 + lc * 16, voffWq = lr * (HQ * DH * 2) + lc * 16;
    nb = 3 + w;
    slot = L_VF + 8192 * w; dslot = 32768;
    auto wq_soff = [&](u32 vp) __attribute__((always_inline)) { u32 v = vp + rot % 7; v = v >= 7 ? v - 7 : v; return v * 64 * (HQ * DH * 2) + w * 128; };
#pragma unroll
    for (int j = 0; j < 2; ++j)
#pragma unroll
      for (int i = 0; i < 8; ++i) dma(rWq, (i & 1) ? (voffWq ^ 64u) : voffWq, wq_soff(j) + i * 8 * (HQ * DH * 2), slot + j * 32768 + i * 1024);
#pragma unroll
    for (int i = 0; i < 4; ++i) ab[i] += slot;
#pragma unroll
    for (int dt = 0; dt < 4; ++dt)
#pragma unroll
      for (int qt = 0; qt < 5; ++qt) acc[dt][qt] = f32x4{0.f, 0.f, 0.f, 0.f};
    wait_vm<12>();
    readA(A0, ab, 0);
    bf16x8 B0[5], B1[5];
    {
      u32 xb = L_END - 10240 + (q >> 3) * 1024 + (q & 7) * 128 + 16 * (g ^ (q & 6));
      for (u32 vp = 0; vp < 6; ++vp) {
        if (vp == 0 || vp == 2 || vp == 5) bar();
        const bool c = vp < 5;
        readB<2048>(B0, B1, xb, xb ^ 64);
        gstep<2048, 8, 10, true, true>(acc, A0, B0, B1, ab, slot, dslot, c ? rWq : rV, c ? wq_soff(vp + 2) : kv_tile(cu, 0) * 64 * 512 + w * 128,
                                       c ? 8 * (HQ * DH * 2) : 8 * 512, c ? voffWq : voffV);
        xb -= 10240;
      }
      {
        readB<2048>(B0, B1, xb, xb ^ 64);
        gstep<2048, 8, 10, true, false>(acc, A0, B0, B1, ab, slot, dslot, rV, kv_tile(cu, 1) * 64 * 512 + w * 128, 8 * 512, voffV);
      }
      const float one[5] = {1.f, 1.f, 1.f, 1.f, 1.f};
      writeB(acc, one, L_QO + w * 10240, lane);
      bar_lds();
      bar();
      if (w == 0 && lane < 2) *(LAS u32*)(L_FLAG + lane * 4) = 0;
#pragma unroll
      for (int i = 0; i < 4; ++i) ab[i] -= dslot;
      wait_vm<12>();
      readA(A0, ab, 0);
    }
#pragma unroll
    for (int dt = 0; dt < 4; ++dt)
#pragma unroll
      for (int qt = 0; qt < 5; ++qt) acc[dt][qt] = f32x4{0.f, 0.f, 0.f, 0.f};
    float mref[5];
#pragma unroll
    for (int qt = 0; qt < 5; ++qt) mref[qt] = -__builtin_inff();
    auto fixup = [&]() __attribute__((always_inline)) {
      bar_lds();
#pragma unroll
      for (int qt = 0; qt < 5; ++qt) {
        const f32x4 m4 = ldf4(L_MX + (qt * 16 + q) * 16);
        const float mn = fmaxf(fmaxf(fmaxf(m4[0], m4[1]), fmaxf(m4[2], m4[3])), mref[qt]);
        const float f = __builtin_amdgcn_exp2f(mref[qt] - mn); mref[qt] = mn;
#pragma unroll
        for (int dt = 0; dt < 4; ++dt) acc[dt][qt] *= f;
      }
      bar_lds();
    };
    fixup();
    u32 pb = L_P + lane * 16; int dp = 10240;
    for (u32 s = 0; s < 15; ++s) {
      const bool c = s < 14;
      readB<1024>(B0, B1, pb, pb + 5120);
      gstep<1024, 8, 10, true, true>(acc, A0, B0, B1, ab, slot, dslot, c ? rV : rWo,
                                     c ? kv_tile(cu, s + 2) * 64 * 512 + w * 128 : (rot & 3) * 64 * (DM * 2) + nb * 128, c ? 8 * 512 : 8 * (DM * 2), c ? voffV : voffWo);
      pb += dp; dp = -dp;
      bar();
      const u32 fl = __builtin_amdgcn_readfirstlane(*(volatile LAS u32*)(L_FLAG + ((s + 1) & 1) * 4));
      if (__builtin_expect(fl != 0, 0)) fixup();
    }
    {
      readB<1024>(B0, B1, pb, pb + 5120);
      bar_lds();
      gstep<1024, 8, 10, true, false>(acc, A0, B0, B1, ab, slot, dslot, rWo, ((rot + 1) & 3) * 64 * (DM * 2) + nb * 128, 8 * (DM * 2), voffWo);
    }
    float inv[5];
#pragma unroll
    for (int qt = 0; qt < 5; ++qt) { const f32x4 l4 = ldf4(L_LS + (qt * 16 + q) * 16); inv[qt] = 1.0f / ((l4[0] + l4[1]) + (l4[2] + l4[3])); }
    writeB(acc, inv, L_QO + w * 10240, lane);
    bar_lds();
#pragma unroll
    for (int i = 0; i < 4; ++i) ab[i] -= dslot;
    wait_vm<12>();
    readA(A0, ab, 0);
  }
  if (wid != 3) {
#pragma unroll
    for (int dt = 0; dt < 4; ++dt)
#pragma unroll
      for (int qt = 0; qt < 5; ++qt) acc[dt][qt] = f32x4{0.f, 0.f, 0.f, 0.f};
    bf16x8 B0[5], B1[5];
    auto ob = [&](u32 j) __attribute__((always_inline)) { return L_QO + ((rot + j) & 3) * 10240 + lane * 16; };
    readB<1024>(B0, B1, ob(0), ob(0) + 5120);
    gstep<1024, 8, 10, true, true>(acc, A0, B0, B1, ab, slot, dslot, rWo, ((rot + 2) & 3) * 64 * (DM * 2) + nb * 128, 8 * (DM * 2), voffWo);
    readB<1024>(B0, B1, ob(1), ob(1) + 5120);
    gstep<1024, 8, 10, true, true>(acc, A0, B0, B1, ab, slot, dslot, rWo, ((rot + 3) & 3) * 64 * (DM * 2) + nb * 128, 8 * (DM * 2), voffWo);
    readB<1024>(B0, B1, ob(2), ob(2) + 5120);
    gstep<1024, 8, 4, false, true>(acc, A0, B0, B1, ab, slot, dslot, rWo, 0, 0, 0);
    readB<1024>(B0, B1, ob(3), ob(3) + 5120);
    gstep<1024, 0, 0, false, false>(acc, A0, B0, B1, ab, slot, dslot, rWo, 0, 0, 0);
    u32x4* dst = part + ((size_t)((2 * b + half) * HQ + head) * 70 + nb * 10) * 64 + lane;
#pragma unroll
    for (int qt = 0; qt < 5; ++qt)
#pragma unroll
      for (int np = 0; np < 2; ++np) {
        const f32x4 a = acc[2 * np][qt], c = acc[2 * np + 1][qt];
        u32x4 wv = {pk(a[0], a[1]), pk(a[2], a[3]), pk(c[0], c[1]), pk(c[2], c[3])};
        dst[(qt * 2 + np) * 64] = wv;
      }
  }
}

__global__ __launch_bounds__(256) void reduce_heads(const u32x4* __restrict__ part, u16* __restrict__ out) {
  const u32 t = blockIdx.x * 256 + threadIdx.x;
  const u32 lane = t & 63, unit = (t >> 6) % 70, grp = (t >> 6) / 70;
  const u32x4* src = part + ((size_t)grp * HQ * 70 + unit) * 64 + lane;
  float a[8] = {0.f, 0.f, 0.f, 0.f, 0.f, 0.f, 0.f, 0.f};
  u32x4 v[16];
#pragma unroll
  for (int h = 0; h < 16; ++h) v[h] = src[(size_t)h * 70 * 64];
#pragma unroll
  for (int h = 0; h < 16; ++h)
#pragma unroll
    for (int i = 0; i < 4; ++i) { a[2 * i] += __uint_as_float(v[h][i] << 16); a[2 * i + 1] += __uint_as_float(v[h][i] & 0xffff0000u); }
  const u32 nb = unit / 10, r = unit % 10, qt = r >> 1, np = r & 1, q = lane & 15, g = lane >> 4;
  u16* o = out + (size_t)(grp * ROWS + qt * 16 + q) * DM + nb * 64 + np * 32 + 4 * g;
  *(u32x2*)o = u32x2{pk(a[0], a[1]), pk(a[2], a[3])};
  *(u32x2*)(o + 16) = u32x2{pk(a[4], a[5]), pk(a[6], a[7])};
}

extern "C" void kernel_launch(void* const* d_in, const int* in_sizes, int n_in, void* d_out, int out_size, void* d_ws, size_t ws_size,
                              hipStream_t stream) {
  (void)in_sizes; (void)n_in; (void)out_size; (void)ws_size;
  (void)hipFuncSetAttribute((const void*)fused_attn, hipFuncAttributeMaxDynamicSharedMemorySize, (int)L_END);
  hipLaunchKernelGGL(fused_attn, dim3(256), dim3(512), L_END, stream, (const u16*)d_in[0], (const u16*)d_in[1], (const u16*)d_in[2],
                     (const u16*)d_in[3], (const u16*)d_in[4], (u32x4*)d_ws);
  hipLaunchKernelGGL(reduce_heads, dim3(280), dim3(256), 0, stream, (const u32x4*)d_ws, (u16*)d_out);
}
```

```cpp
#include <hip/hip_runtime.h>
#include <stdint.h>
#include <utility>

typedef unsigned int u32;
typedef unsigned short u16;
typedef __bf16 bf16x8_t __attribute__((ext_vector_type(8)));
typedef __bf16 bf16x2 __attribute__((ext_vector_type(2)));
typedef short s16x4 __attribute__((ext_vector_type(4)));
typedef float f32x4 __attribute__((ext_vector_type(4)));
typedef float f32x2 __attribute__((ext_vector_type(2)));
typedef u32 u32x2 __attribute__((ext_vector_type(2)));
typedef u32 u32x4 __attribute__((ext_vector_type(4)));
typedef u32x4 rsrc_t;
typedef u32x4 bf16x8;
#define LAS __attribute__((address_space(3)))
#define DI __device__ __forceinline__
#define AI __attribute__((always_inline))
#define SBAR() __builtin_amdgcn_sched_barrier(0)

template <class F, int... Is> DI void sfor_(F&& f, std::integer_sequence<int, Is...>) { (f(std::integral_constant<int, Is>{}), ...); }
template <int N, class F> DI void sfor(F&& f) { sfor_(f, std::make_integer_sequence<int, N>{}); }

constexpr int SQ = 160, DM = 448, HQ = 16, HKV = 8, SK = 1024, DH = 256;
constexpr int ROWS = 80;
constexpr float SCL = 0.0625f * 1.4426950408889634f;
constexpr float PTHR = 1048576.0f;
constexpr float LTHR = 18.0f;

constexpr u32 L_VF = 0;
constexpr u32 L_KC = 65536;
constexpr u32 L_X = 90112;
constexpr u32 L_P = 131072;
constexpr u32 L_MX = 141312;
constexpr u32 L_FLAG = 143872;
constexpr u32 L_LS = L_KC + 6 * 8192;
constexpr u32 L_O = 122880;
constexpr u32 L_END = 163840;

DI void dma(rsrc_t r, u32 voff, u32 soff, u32 ldsaddr) {
  asm volatile("s_mov_b32 m0, %0\n\ts_nop 0\n\tbuffer_load_dwordx4 %1, %2, %3 offen lds" ::"s"(ldsaddr), "v"(voff), "s"(r), "s"(soff) : "memory");
}
template <int LOFF> DI void set_m0(u32 lds) { asm volatile("s_add_u32 m0, %0, %1" ::"s"(lds), "i"(LOFF) : "memory", "scc"); }
template <int IMM> DI void dma_m0(rsrc_t r, u32 voff, u32 soff) {
  asm volatile("buffer_load_dwordx4 %0, %1, %2 offen offset:%3 lds" ::"v"(voff), "s"(r), "s"(soff), "i"(IMM) : "memory");
}
DI void mfma_acc(f32x4& c, const bf16x8& a, const bf16x8& b) { asm volatile("v_mfma_f32_16x16x32_bf16 %0, %1, %2, %0" : "+v"(c) : "v"(a), "v"(b)); }
DI void mfma_zero(f32x4& d, const bf16x8& a, const bf16x8& b) { asm volatile("v_mfma_f32_16x16x32_bf16 %0, %1, %2, 0" : "+v"(d) : "v"(a), "v"(b)); }
DI void pin4(const bf16x8 (&A)[4]) { asm volatile("" ::"v"(A[0]), "v"(A[1]), "v"(A[2]), "v"(A[3]) : "memory"); }
DI void pin5(const bf16x8 (&A)[5]) { asm volatile("" ::"v"(A[0]), "v"(A[1]), "v"(A[2]), "v"(A[3]), "v"(A[4]) : "memory"); }
template <int N> DI void wait_vm() { asm volatile("s_waitcnt vmcnt(%0)" ::"i"(N) : "memory"); }
DI void bar() { asm volatile("s_barrier" ::: "memory"); }
DI void bar_lds() { asm volatile("s_waitcnt lgkmcnt(0)\n\ts_barrier" ::: "memory"); }
DI bf16x8 ld128(u32 a) { return *(const LAS bf16x8*)a; }
DI f32x4 ldf4(u32 a) { return *(const LAS f32x4*)a; }
DI u32x4 ldu4(u32 a) { return *(const LAS u32x4*)a; }
DI u32x2 ldtr1(u32 a) { return __builtin_bit_cast(u32x2, __builtin_amdgcn_ds_read_tr16_b64_v4i16((LAS s16x4*)a)); }
DI u32 pk(float lo, float hi) { f32x2 v = {lo, hi}; return __builtin_bit_cast(u32, __builtin_convertvector(v, bf16x2)); }
DI f32x4 mfma(bf16x8 a, bf16x8 b, f32x4 c) { return __builtin_amdgcn_mfma_f32_16x16x32_bf16(__builtin_bit_cast(bf16x8_t, a), __builtin_bit_cast(bf16x8_t, b), c, 0, 0, 0); }
DI rsrc_t mkrsrc(const void* p, u32 bytes) { const uint64_t a = (uint64_t)p; return rsrc_t{(u32)a, (u32)(a >> 32) & 0xffffu, bytes, 0x00020000u}; }
DI u32 fresh_lane() { u32 l; asm volatile("v_mbcnt_lo_u32_b32 %0, -1, 0\n\tv_mbcnt_hi_u32_b32 %0, -1, %0" : "=v"(l)); return l; }
DI float ex2(float x) { return __builtin_amdgcn_exp2f(x); }
DI float max3(float a, float b, float c) { float r; asm("v_max3_f32 %0, %1, %2, %3" : "=v"(r) : "v"(a), "v"(b), "v"(c)); return r; }
DI float max2(float a, float b) { float r; asm("v_max_f32_e32 %0, %1, %2" : "=v"(r) : "v"(a), "v"(b)); return r; }
DI float fadd(float a, float b) { float r; asm("v_add_f32_e32 %0, %1, %2" : "=v"(r) : "v"(a), "v"(b)); return r; }
DI float xmax(float v) {
  auto a = __builtin_amdgcn_permlane16_swap(__float_as_uint(v), __float_as_uint(v), false, false);
  v = fmaxf(__uint_as_float(a[0]), __uint_as_float(a[1]));
  auto b = __builtin_amdgcn_permlane32_swap(__float_as_uint(v), __float_as_uint(v), false, false);
  return fmaxf(__uint_as_float(b[0]), __uint_as_float(b[1]));
}
DI float xsum(float v) {
  auto a = __builtin_amdgcn_permlane16_swap(__float_as_uint(v), __float_as_uint(v), false, false);
  v = __uint_as_float(a[0]) + __uint_as_float(a[1]);
  auto b = __builtin_amdgcn_permlane32_swap(__float_as_uint(v), __float_as_uint(v), false, false);
  return __uint_as_float(b[0]) + __uint_as_float(b[1]);
}
DI bf16x8 scale8(bf16x8 u, float f) {
#pragma unroll
  for (int i = 0; i < 4; ++i) {
    const float lo = __uint_as_float(u[i] << 16) * f, hi = __uint_as_float(u[i] & 0xffff0000u) * f;
    u32 r; asm("v_cvt_pk_bf16_f32 %0, %1, %2" : "=v"(r) : "v"(lo), "v"(hi));
    u[i] = r;
  }
  asm volatile("s_nop 1" : "+v"(u));
  return u;
}

#define ST(i)
#define STR(i)
#define ST_OUT()
#define NO_PV 0
#define NO_WR 0
#define KV_BYTES (SK * DH * 2)
#define NO_KDMA 0
#define VDMA 1

DI void readA1(bf16x8 (&A)[4], const u32 (&ab)[4], int e, int i) {
  const int dt = i >> 1;
  const u32x2 v = (i & 1) ? ldtr1(ab[dt ^ 2] + 4096 * e + 512) : ldtr1(ab[dt] + 4096 * e);
  if (i & 1) { A[dt][2] = v[0]; A[dt][3] = v[1]; } else { A[dt][0] = v[0]; A[dt][1] = v[1]; }
}
DI void readA(bf16x8 (&A)[4], const u32 (&ab)[4], int e) {
#pragma unroll
  for (int i = 0; i < 8; ++i) readA1(A, ab, e, i);
}
DI void readB(bf16x8 (&B)[5], u32 bb) {
#pragma unroll
  for (int qt = 0; qt < 5; ++qt) B[qt] = ld128(bb + qt * 2048);
}
constexpr int vpos[4] = {3, 8, 13, 18};
template <bool RF, bool LAST, int W1, int W0, class Mid, class Ep>
DI void gstep(f32x4 (&acc)[4][5], bf16x8 (&A0)[4], bf16x8 (&B0)[5], bf16x8 (&B1)[5], u32 (&ab)[4], u32& slot, u32 b1,
              rsrc_t rr, u32 rsoff, const u32 (&rv)[8], Mid mid, Ep ep) {
  bf16x8 A1[4];
  sfor<20>([&](auto nc) AI {
    constexpr int n = nc.value, j = n == vpos[0] ? 0 : n == vpos[1] ? 1 : n == vpos[2] ? 2 : n == vpos[3] ? 3 : -1;
    if constexpr (RF && j >= 0) { if (j == 0) pin4(A0); set_m0<1024 * (j < 0 ? 0 : j)>(slot); SBAR(); }
    acc[n / 5][n % 5] = mfma(A0[n / 5], B0[n % 5], acc[n / 5][n % 5]);
    SBAR();
    if constexpr (RF && j >= 0 && VDMA) dma_m0<0>(rr, rv[j < 0 ? 0 : j], rsoff);
    if constexpr (RF && VDMA == 2 && n % 5 == 0 && n > 0) dma_m0<0>(rr, rv[n / 5 - 1], rsoff);
    if (n < 5) B1[n % 5] = ld128(b1 + n * 2048);
    if (n == 5) wait_vm<W1>();
    if (n >= 5 && n < 13) readA1(A1, ab, 1, n - 5);
    if (n == 16) { ab[0] ^= 8192u; ab[1] ^= 8192u; asm volatile("" : "+v"(ab[0]), "+v"(ab[1])); }
    if (n == 17) { ab[2] ^= 8192u; ab[3] ^= 8192u; asm volatile("" : "+v"(ab[2]), "+v"(ab[3])); }
    SBAR();
  });
  const u32 b0 = mid();
  SBAR();
  sfor<20>([&](auto nc) AI {
    constexpr int n = nc.value, j = n == vpos[0] ? 4 : n == vpos[1] ? 5 : n == vpos[2] ? 6 : n == vpos[3] ? 7 : -1;
    constexpr int dt = LAST ? n % 4 : n / 5, qt = LAST ? n / 4 : n % 5;
    if constexpr (RF && j >= 0) { if (j == 4) pin4(A1); set_m0<1024 * (j < 0 ? 0 : j)>(slot); SBAR(); }
    acc[dt][qt] = mfma(A1[dt], B1[qt], acc[dt][qt]);
    SBAR();
    if constexpr (RF && j >= 0 && VDMA) dma_m0<0>(rr, rv[j < 0 ? 0 : j], rsoff);
    if constexpr (RF && VDMA == 2 && n % 5 == 0) dma_m0<0>(rr, rv[n / 5 + 3], rsoff);
    if constexpr (RF && VDMA == 2 && n == 19) dma_m0<0>(rr, rv[7], rsoff);
    if constexpr (!LAST) {
      if (n < 5) B0[n % 5] = ld128(b0 + n * 2048);
      if (n == 5) wait_vm<W0>();
      if (n >= 5 && n < 13) readA1(A0, ab, 0, n - 5);
    } else if constexpr (n >= 2) ep(std::integral_constant<int, (n - 2) % 4>{}, std::integral_constant<int, (n - 2) / 4>{});
    SBAR();
  });
  if constexpr (LAST) { ep(std::integral_constant<int, 2>{}, std::integral_constant<int, 4>{}); ep(std::integral_constant<int, 3>{}, std::integral_constant<int, 4>{}); }
  slot ^= 8192u;
}
struct NoEp { template <class A, class B> DI void operator()(A, B) const {} };

constexpr int kpos[8] = {6, 11, 16, 21, 26, 31, 36, 38};
constexpr int kcount(int n) { int c = 0; for (int j = 0; j < 8; ++j) c += kpos[j] < n; return c; }
constexpr u32 kv_tile(u32 cu, u32 s) { const u32 d = (cu - s) & 3; const u32 g = d == 0 ? 0 : (d == 1 ? 2 : 3); return (s - g) & 15; }
constexpr uint64_t kv_seq(u32 cu) { uint64_t t = 0; for (u32 i = 0; i < 16; ++i) t |= (uint64_t)kv_tile(cu, i) << (4 * i); return t; }

__global__ __launch_bounds__(512, 2) void fused_attn(const u16* __restrict__ x, const u16* __restrict__ kc, const u16* __restrict__ vc,
                                                     const u16* __restrict__ wq, const u16* __restrict__ wo, u32x4* __restrict__ part) {
  const u32 lane = threadIdx.x & 63, wid = __builtin_amdgcn_readfirstlane(threadIdx.x >> 6), w = wid & 3;
  const u32 blk = blockIdx.x, kvh = blk & 7, idx = blk >> 3, b = idx >> 2, cu = idx & 3, head = 2 * kvh + (cu >> 1), half = cu & 1;
  ST(0); STR(40);
  const u32 q = lane & 15, g = lane >> 4;
  const u32 lr = lane >> 3, lc = (lane & 7) ^ (lr & 6);
  const u32 rot = 2 * b + half, rot7 = rot % 7;
  const uint64_t tseq = cu == 0 ? kv_seq(0) : cu == 1 ? kv_seq(1) : cu == 2 ? kv_seq(2) : kv_seq(3);
  auto tile_off = [&](u32 s) AI { return ((u32)(tseq >> (4 * s)) & 15) * (64 * 512); };
  const u32 nb = wid < 4 ? w : 3 + w;
  auto wo_soff = [&](u32 j) AI { return ((rot + j) & 3) * 64 * (DM * 2) + nb * 128; };

  f32x4 acc[4][5];
  bf16x8 A0[4], B0[5], B1[5];
  u32 ab[4], rv[8], slot = wid < 4 ? L_KC + 16384 * w : L_VF + 16384 * w;
  auto init_ab = [&]() AI {
    const u32 lane = fresh_lane(), g = lane >> 4;
    const u32 q4 = (lane & 15) >> 2, p = lane & 3, f0 = (q4 & 2) ^ ((g & 1) << 2);
#pragma unroll
    for (int i = 0; i < 4; ++i) ab[i] = slot + g * 1024 + q4 * 128 + 16 * (((2 * i) ^ f0) | (p >> 1)) + 8 * (p & 1);
  };
  auto init_rv = [&](u32 stride) AI {
    const u32 lane = fresh_lane(), lr = lane >> 3, lc = (lane & 7) ^ (lr & 6);
#pragma unroll
    for (int i = 0; i < 8; ++i) rv[i] = (lr + 8 * i) * stride + ((lc * 16) ^ ((i & 1) * 64));
  };
  auto issue = [&](rsrc_t rr, u32 so, u32 dst) AI {
#pragma unroll
    for (int i = 0; i < 8; ++i) dma(rr, rv[i], so, dst + i * 1024);
  };
  auto zero_acc = [&]() AI {
#pragma unroll
    for (int dt = 0; dt < 4; ++dt)
#pragma unroll
      for (int qt = 0; qt < 5; ++qt) acc[dt][qt] = f32x4{0.f, 0.f, 0.f, 0.f};
  };
  const rsrc_t rWo = mkrsrc(wo + (size_t)head * DH * DM, DH * DM * 2);

  if (wid < 4) {
    {
      const rsrc_t rX = mkrsrc(x + (size_t)(b * SQ + half * ROWS) * DM, ROWS * DM * 2);
      const u32 voffX = lr * (DM * 2) + lc * 16;
      u32 v = rot7, dst = L_X;
#pragma unroll 1
      for (u32 vp = 0; vp < 7; ++vp) {
        dma(rX, voffX, w * 8 * (DM * 2) + v * 128, dst + w * 1024);
        dma(rX, voffX, (w + 4) * 8 * (DM * 2) + v * 128, dst + (w + 4) * 1024);
        if (w < 2) { dma(rX, voffX, (w + 8) * 8 * (DM * 2) + v * 128, dst + (w + 8) * 1024); wait_vm<3>(); } else wait_vm<2>();
        if (vp > 0) bar();
        v = v == 6 ? 0 : v + 1; dst += 10240;
      }
    }
    const rsrc_t rK = mkrsrc(kc + (size_t)(b * HKV + kvh) * SK * DH, KV_BYTES);
    const u32 vK0 = lr * 512 + lc * 16, vK1 = vK0 + 4096;
    ST(1);
    wait_vm<0>();
    bar();
    ST(2);
#pragma unroll 1
    for (u32 j = 0; j < 2; ++j) {
      const u32 so = tile_off(j) + 16 * w * 512, ks = slot + j * 8192;
#pragma unroll
      for (int v = 0; v < 4; ++v) { dma(rK, vK0, so + 128 * v, ks + v * 1024); dma(rK, vK1, so + 128 * v, ks + 4096 + v * 1024); }
    }
    u32 kb0 = slot + (q >> 3) * 4096 + (q & 7) * 128 + 16 * (g ^ (q & 6)), kb1 = kb0 ^ 64;
    u32 pw = L_P + (((2 * w + (g >> 1)) & 3) * 16 + q) * 16 + (g & 1) * 8 + (w >> 1) * 1024;
    float mref[5], l[5];
#pragma unroll
    for (int qt = 0; qt < 5; ++qt) { mref[qt] = 0.f; l[qt] = 0.f; }
    f32x4 S[2][5];
    bf16x8 kf[4];
    auto rdK = [&](int u) AI { return ld128(((u & 1) ? kb1 : kb0) + (u >> 1) * 1024); };
    ST(5);
    bar();
    ST(6);
    bf16x8 Q[8][5];
    auto rdQ = [&](int u, int qt) AI { Q[u][qt] = ld128(lane * 16 + (qt < 4 ? L_VF + 16384 * (u >> 1) + qt * 2048 : L_P + 2048 * (u >> 1)) + (u & 1) * 1024); };
    wait_vm<8>();
    sfor<8>([&](auto uc) AI {
      constexpr int u = uc.value;
      if (u == 0) { kf[0] = rdK(0); rdQ(0, 0); rdQ(0, 1); rdQ(0, 2); rdQ(0, 3); rdQ(0, 4); }
      if (u < 7) { kf[(u + 1) & 1] = rdK(u + 1); rdQ((u + 1) & 7, 0); rdQ((u + 1) & 7, 1); rdQ((u + 1) & 7, 2); rdQ((u + 1) & 7, 3); rdQ((u + 1) & 7, 4); }
#pragma unroll
      for (int qt = 0; qt < 5; ++qt) S[0][qt] = mfma(kf[u & 1], Q[u][qt], u == 0 ? f32x4{0.f, 0.f, 0.f, 0.f} : S[0][qt]);
      SBAR();
    });
#pragma unroll
    for (int qt = 0; qt < 5; ++qt) S[1][qt] = f32x4{0.f, 0.f, 0.f, 0.f};
    u32 fla = L_FLAG + 16 + (lane & 3) * 4;
    const u32 flw = (w - (lane & 3)) * 4;
    *(LAS u32*)(fla + flw) = 0;
    bar_lds();
    {
      const u32 so = tile_off(2) + 16 * w * 512;
#pragma unroll
      for (int v = 0; v < 4; ++v) { dma(rK, vK0, so + 128 * v, slot + v * 1024); dma(rK, vK1, so + 128 * v, slot + 4096 + v * 1024); }
    }
    kb0 ^= 8192u; kb1 ^= 8192u; slot ^= 8192u;
    wait_vm<12>();
    kf[0] = rdK(0); kf[1] = rdK(1); kf[2] = rdK(2);
    ST(7);

    auto adopt = [&](u32 par, bool s0) AI {
      const u32x4 f4 = ldu4(L_FLAG + par * 16);
      const u32 q = fresh_lane() & 15;
#pragma unroll
      for (int qt = 0; qt < 5; ++qt) {
        const f32x4 m4 = ldf4(L_MX + par * 1280 + (qt * 16 + q) * 16);
        const float own = mref[qt], base = s0 ? 0.f : own;
        float M = own;
#pragma unroll
        for (int i = 0; i < 4; ++i) M = fmaxf(M, f4[i] ? m4[i] : base);
        l[qt] *= ex2(own - M); mref[qt] = M;
        asm volatile("" : "+v"(l[qt]), "+v"(mref[qt]));
        SBAR();
      }
    };
    float t[4], ps[5];
    u32x2 pkd;
    float thr = -1.f;
    const u32 zero = 0;
    auto step = [&](auto xc, auto mc, u32 s) AI {
      constexpr int X = xc.value; constexpr bool MM = mc.value;
      u32 fl, so; float chk; uint64_t mask;
      sfor<40>([&](auto nc) AI {
        constexpr int n = nc.value, u = n / 5, qt = n % 5;
        constexpr bool kd = MM && kcount(n + 1) != kcount(n) && !NO_KDMA;
        constexpr int j = kcount(n), jv = j >> 1, jh = j & 1;
        if constexpr (kd) set_m0<jh * 4096 + jv * (1024 - 128)>(slot);
        if constexpr (MM) { if (u == 0) mfma_zero(S[X ^ 1][qt], kf[u & 3], Q[u][qt]); else mfma_acc(S[X ^ 1][qt], kf[u & 3], Q[u][qt]); }
        if constexpr (kd) dma_m0<128 * jv>(rK, jh ? vK1 : vK0, so);
        SBAR();
        if constexpr (MM && qt == 0) {
          if constexpr (u == 1 || u == 3) wait_vm<8 + 2 * (3 - (u + 3) / 2) + kcount(n + 1)>();
          if constexpr (u == 5 || u == 7) wait_vm<2 * (3 - (u - 5) / 2) + kcount(n + 1)>();
          kf[(u + 3) & 3] = rdK((u + 3) & 7);
        }
        if (n == 1) fl = *(const LAS u32*)fla;
        if (MM && n == 2) {
          so = (s < 13 ? tile_off((s + 3) & 15) : (u32)(SK * DH * 2)) + 16 * w * 512;
          asm volatile("" : "+s"(so));
        }
        if (MM && n == 22) { kb0 ^= 8192u; kb1 ^= 8192u; }
        if constexpr (n >= 1 && n < 31) {
          constexpr int c = (n - 1) / 6, k = (n - 1) % 6;
          if (k == 0) { t[0] = fmaf(S[X][c][0], SCL, -mref[c]); t[1] = fmaf(S[X][c][1], SCL, -mref[c]); asm volatile("" : "+v"(t[0])); asm volatile("" : "+v"(t[1])); }
          if (k == 1) { t[2] = fmaf(S[X][c][2], SCL, -mref[c]); t[0] = ex2(t[0]); t[3] = fmaf(S[X][c][3], SCL, -mref[c]); asm volatile("" : "+v"(t[2])); asm volatile("" : "+v"(t[3])); }
          if (k == 2) { t[1] = ex2(t[1]); t[2] = ex2(t[2]); }
          if (k == 3) { t[3] = ex2(t[3]); pkd[0] = pk(t[0], t[1]); t[0] = fadd(t[0], t[1]); }
          if (k == 4) { pkd[1] = pk(t[2], t[3]); t[2] = fadd(t[2], t[3]); }
          if (k == 5) { if (NO_WR) asm volatile("" :: "v"(pkd)); else *(LAS u32x2*)(pw + c * 2048) = pkd; ps[c] = fadd(t[0], t[2]); }
        }
        if (n == 26) { asm volatile("" : "+v"(fl)); chk = max3(ps[0], ps[1], __uint_as_float(fl)); fla ^= 16u; }
        if (n == 28) { chk = max3(chk, ps[2], ps[3]); if (!NO_WR || s < 2) *(LAS u32*)(fla + flw) = zero; }
        if (n == 31) chk = max2(chk, ps[4]);
        if (n == 32) { mask = __builtin_amdgcn_ballot_w64(chk > thr); asm volatile("" : "+s"(mask)); }
        if (n == 33) {
          if (__builtin_expect(mask != 0, 0)) {
            const bool ad = __any(fl != 0), s0 = s == 0;
            if (ad) adopt((s + 1) & 1, s == 1);
            float mm = -__builtin_inff(), mn = __builtin_inff();
#pragma unroll
            for (int c = 0; c < 5; ++c) {
              const float m = fmaf(fmaxf(fmaxf(S[X][c][0], S[X][c][1]), fmaxf(S[X][c][2], S[X][c][3])), SCL, -mref[c]);
              mm = fmaxf(mm, m); mn = fminf(mn, m);
            }
            const bool move = __any(mm > LTHR || (s0 && mn < -LTHR));
            if (move || ad) {
              const u32 q = fresh_lane() & 15;
#pragma unroll
              for (int c = 0; c < 5; ++c) {
                float d = __builtin_ceilf(xmax(fmaf(fmaxf(fmaxf(S[X][c][0], S[X][c][1]), fmaxf(S[X][c][2], S[X][c][3])), SCL, -mref[c])));
                d = move ? (s0 ? d : fmaxf(d, 0.f)) : 0.f;
                l[c] *= ex2(fminf(-d, 0.f)); mref[c] += d;
                const float p0 = ex2(fmaf(S[X][c][0], SCL, -mref[c])), p1 = ex2(fmaf(S[X][c][1], SCL, -mref[c]));
                const float p2 = ex2(fmaf(S[X][c][2], SCL, -mref[c])), p3 = ex2(fmaf(S[X][c][3], SCL, -mref[c]));
                *(LAS u32x2*)(pw + c * 2048) = u32x2{pk(p0, p1), pk(p2, p3)};
                ps[c] = (p0 + p1) + (p2 + p3);
                *(LAS float*)(L_MX + (s & 1) * 1280 + (c * 16 + q) * 16 + w * 4) = mref[c];
                asm volatile("" : "+v"(ps[c]), "+v"(l[c]), "+v"(mref[c]));
                SBAR();
              }
              if (move) *(LAS u32*)(fla + flw) = 0x7f800000u;
            }
            thr = PTHR;
          }
        }
        if (n == 34) { l[0] = fadd(l[0], ps[0]); l[1] = fadd(l[1], ps[1]); }
        if (n == 36) { l[2] = fadd(l[2], ps[2]); l[3] = fadd(l[3], ps[3]); }
        if (n == 37) { l[4] = fadd(l[4], ps[4]); pw ^= 16384u; asm volatile("" : "+v"(pw)); }
        if (MM && n == 39) { slot ^= 8192u; asm volatile("" : "+s"(slot)); }
        SBAR();
      });
      if constexpr (MM) asm volatile("s_waitcnt lgkmcnt(1)\n\ts_barrier" ::: "memory");
      else bar_lds();
    };
    using I0 = std::integral_constant<int, 0>; using I1 = std::integral_constant<int, 1>;
    step(I0{}, I1{}, 0);
    ST(9); STR(41);
#pragma unroll 1
    for (u32 s = 1; s < 15; s += 2) {
      step(I1{}, I1{}, s); step(I0{}, I1{}, s + 1);
    }
    ST(10); STR(42);
    wait_vm<0>();
    asm volatile("s_nop 7" ::: "memory");
    step(I1{}, I0{}, 15);
    slot = L_KC + 16384 * w;
    if (__any(*(const LAS u32*)fla != 0)) adopt(1, false);
    { const u32 q = fresh_lane() & 15;
#pragma unroll
      for (int qt = 0; qt < 5; ++qt) *(LAS float*)(L_LS + (qt * 16 + q) * 16 + w * 4) = xsum(l[qt]);
    }
    init_rv(DM * 2);
    if (w < 3) { issue(rWo, wo_soff(0), slot); issue(rWo, wo_soff(1), slot + 8192); }
    bar_lds();
    init_ab();
    wait_vm<12>();
    readA(A0, ab, 0);
    zero_acc();
    ST(58);
    bar();
    ST(59);
  } else {
    const rsrc_t rWq = mkrsrc(wq + head * DH, (DM * HQ * DH - head * DH) * 2);
    auto wq_soff = [&](u32 vp) AI { u32 v = vp + rot7; v = v >= 7 ? v - 7 : v; return v * 64 * (HQ * DH * 2) + w * 128; };
    init_rv(HQ * DH * 2);
    issue(rWq, wq_soff(0), slot);
    init_ab();
    zero_acc();
    SBAR();
    issue(rWq, wq_soff(1), slot + 8192);
    const rsrc_t rV = mkrsrc(vc + (size_t)(b * HKV + kvh) * SK * DH, KV_BYTES);
    const u32 wa = ((g >> 1) * 16 + q) * 16 + (g & 1) * 8;
    wait_vm<12>();
    readA(A0, ab, 0);
    {
      u32 xb = L_X + (q >> 3) * 1024 + (q & 7) * 128 + 16 * (g ^ (q & 6));
      bar();
      ST(1);
      readB(B0, xb);
      auto mid = [&]() AI { pin5(B1); bar();   xb += 10240; return xb; };
#pragma unroll 1
      for (u32 vp = 0; vp < 5; ++vp) gstep<true, false, 9, 9>(acc, A0, B0, B1, ab, slot, xb ^ 64, rWq, wq_soff(vp + 2), rv, mid, NoEp{});
      ST(2);
      gstep<false, false, 8, 4>(acc, A0, B0, B1, ab, slot, xb ^ 64, rWq, 0, rv, mid, NoEp{});
      ST(3);
      const u32 qa = slot + wa, qa2 = L_P + 2048 * w + wa;
      gstep<false, true, 0, 0>(acc, A0, B0, B1, ab, slot, xb ^ 64, rWq, 0, rv, [&]() AI { return 0u; }, [&](auto dtc, auto qtc) AI {
        constexpr int dt = dtc.value, qt = qtc.value;
        const f32x4 v = acc[dt][qt];
        *(LAS u32x2*)((qt < 4 ? qa + qt * 2048 : qa2) + (dt >> 1) * 1024 + (dt & 1) * 512) = u32x2{pk(v[0], v[1]), pk(v[2], v[3])};
      });
      ST(4);
      bar_lds();
      init_rv(512);
      issue(rV, tile_off(0) + w * 128, slot);
      zero_acc();
      bar();
      issue(rV, tile_off(1) + w * 128, slot ^ 8192u);
      wait_vm<12>();
      readA(A0, ab, 0);
      ST(5);
    }
    float mref[5];
#pragma unroll
    for (int qt = 0; qt < 5; ++qt) mref[qt] = 0.f;
    u32 pb = L_P + lane * 16;
    u32 fla = L_FLAG + (lane & 3) * 4;
    bar();
    ST(6);
    u32 fl = *(const LAS u32*)fla;
    readB(B0, pb);
    rsrc_t rr = rV;
    float f1[5];
    bool rare;
    auto pre = [&](u32 s) AI {
      rare = __any(fl != 0);
      if (__builtin_expect(rare, 0)) {
        const u32x4 f4 = ldu4(L_FLAG + (s & 1) * 16);
#pragma unroll
        for (int qt = 0; qt < 5; ++qt) {
          const f32x4 m4 = ldf4(L_MX + (s & 1) * 1280 + (qt * 16 + q) * 16);
          float r[4];
#pragma unroll
          for (int i = 0; i < 4; ++i) r[i] = f4[i] ? m4[i] : mref[qt];
          const float M = fmaxf(fmaxf(r[0], r[1]), fmaxf(r[2], r[3]));
          const float f = ex2(fminf(mref[qt] - M, 0.f));
#pragma unroll
          for (int dt = 0; dt < 4; ++dt) acc[dt][qt] *= f;
          B0[qt] = scale8(B0[qt], ex2(((g >> 1) ? r[1] : r[0]) - M));
          f1[qt] = ex2(((g >> 1) ? r[3] : r[2]) - M);
          mref[qt] = M;
        }
      }
    };
    auto mid = [&]() AI {
      pb ^= 16384u; fla ^= 16u;
      pin5(B1);
      if (__builtin_expect(rare, 0)) {
#pragma unroll
        for (int qt = 0; qt < 5; ++qt) B1[qt] = scale8(B1[qt], f1[qt]);
      }
      bar();
    };
#pragma unroll 1
    for (u32 s = 0; s < 15; ++s) {
      if (NO_PV) { bar(); continue; }
      u32 rsoff = tile_off((s + 2) & 15) + w * 128;
      if (__builtin_expect(s == 14, 0)) { rr = rWo; rsoff = wo_soff(0); init_rv(DM * 2); }
      pre(s);
      gstep<true, false, 9, 9>(acc, A0, B0, B1, ab, slot, pb + 1024, rr, rsoff, rv, [&]() AI { mid(); fl = *(const LAS u32*)fla; return pb; }, NoEp{});
    }
    ST(56);
    pre(15);
    float inv[5];
    const u32 oa = L_O + w * 10240 + wa;
    gstep<true, true, 9, 0>(acc, A0, B0, B1, ab, slot, pb + 1024, rWo, wo_soff(1), rv, [&]() AI {
      mid();
#pragma unroll
      for (int qt = 0; qt < 5; ++qt) { const f32x4 l4 = ldf4(L_LS + (qt * 16 + q) * 16); inv[qt] = __builtin_amdgcn_rcpf((l4[0] + l4[1]) + (l4[2] + l4[3])); }
      return 0u;
    }, [&](auto dtc, auto qtc) AI {
      constexpr int dt = dtc.value, qt = qtc.value;
      const f32x4 v = acc[dt][qt] * inv[qt];
      *(LAS u32x2*)(oa + qt * 2048 + (dt >> 1) * 1024 + (dt & 1) * 512) = u32x2{pk(v[0], v[1]), pk(v[2], v[3])};
    });
    wait_vm<12>();
    readA(A0, ab, 0);
    zero_acc();
    ST(58);
    asm volatile("s_waitcnt lgkmcnt(8)\n\ts_barrier" ::: "memory");
    ST(59);
  }
  if (wid != 3) {
    const u32 lane = fresh_lane();
    slot = wid < 4 ? L_KC + 16384 * w : L_VF + 16384 * w + 8192;
    auto ob = [&](u32 j) AI { return L_O + ((rot + j) & 3) * 10240 + lane * 16; };
    readB(B0, ob(0));
#pragma unroll 1
    for (u32 j = 0; j < 2; ++j)
      gstep<true, false, 9, 9>(acc, A0, B0, B1, ab, slot, ob(j) + 1024, rWo, wo_soff(j + 2), rv, [&]() AI { return ob(j + 1); }, NoEp{});
    gstep<false, false, 8, 4>(acc, A0, B0, B1, ab, slot, ob(2) + 1024, rWo, 0, rv, [&]() AI { return ob(3); }, NoEp{});
    ST(60);
    u32x4* dst = part + ((size_t)((2 * b + half) * HQ + head) * 70 + nb * 10) * 64 + lane;
    gstep<false, true, 0, 0>(acc, A0, B0, B1, ab, slot, ob(3) + 1024, rWo, 0, rv, [&]() AI { return 0u; }, [&](auto dtc, auto qtc) AI {
      constexpr int dt = dtc.value, qt = qtc.value;
      if constexpr (dt & 1) {
        const f32x4 a = acc[dt - 1][qt], c = acc[dt][qt];
        dst[(qt * 2 + (dt >> 1)) * 64] = u32x4{pk(a[0], a[1]), pk(a[2], a[3]), pk(c[0], c[1]), pk(c[2], c[3])};
      }
    });
  }
  ST_OUT();
}

__global__ __launch_bounds__(256) void reduce_heads(const u32x4* __restrict__ part, u16* __restrict__ out) {
  const u32 t = blockIdx.x * 256 + threadIdx.x;
  const u32 lane = t & 63, unit = (t >> 6) % 70, grp = (t >> 6) / 70;
  const u32x4* src = part + ((size_t)grp * HQ * 70 + unit) * 64 + lane;
  float a[8] = {0.f, 0.f, 0.f, 0.f, 0.f, 0.f, 0.f, 0.f};
  u32x4 v[16];
#pragma unroll
  for (int h = 0; h < 16; ++h) v[h] = src[(size_t)h * 70 * 64];
#pragma unroll
  for (int h = 0; h < 16; ++h)
#pragma unroll
    for (int i = 0; i < 4; ++i) { a[2 * i] += __uint_as_float(v[h][i] << 16); a[2 * i + 1] += __uint_as_float(v[h][i] & 0xffff0000u); }
  const u32 nb = unit / 10, r = unit % 10, qt = r >> 1, np = r & 1, q = lane & 15, g = lane >> 4;
  u16* o = out + (size_t)(grp * ROWS + qt * 16 + q) * DM + nb * 64 + np * 32 + 4 * g;
  *(u32x2*)o = u32x2{pk(a[0], a[1]), pk(a[2], a[3])};
  *(u32x2*)(o + 16) = u32x2{pk(a[4], a[5]), pk(a[6], a[7])};
}

extern "C" void kernel_launch(void* const* d_in, const int* in_sizes, int n_in, void* d_out, int out_size, void* d_ws, size_t ws_size,
                              hipStream_t stream) {
  (void)in_sizes; (void)n_in; (void)out_size; (void)ws_size;
  (void)hipFuncSetAttribute((const void*)fused_attn, hipFuncAttributeMaxDynamicSharedMemorySize, (int)L_END);
  hipLaunchKernelGGL(fused_attn, dim3(256), dim3(512), L_END, stream, (const u16*)d_in[0], (const u16*)d_in[1], (const u16*)d_in[2],
                     (const u16*)d_in[3], (const u16*)d_in[4], (u32x4*)d_ws);
  hipLaunchKernelGGL(reduce_heads, dim3(280), dim3(256), 0, stream, (const u32x4*)d_ws, (u16*)d_out);
}
```

```cpp
#include <hip/hip_runtime.h>
#include <stdint.h>
#include <utility>

typedef unsigned int u32;
typedef unsigned short u16;
typedef __bf16 bf16x8_t __attribute__((ext_vector_type(8)));
typedef __bf16 bf16x2 __attribute__((ext_vector_type(2)));
typedef short s16x4 __attribute__((ext_vector_type(4)));
typedef float f32x4 __attribute__((ext_vector_type(4)));
typedef float f32x2 __attribute__((ext_vector_type(2)));
typedef u32 u32x2 __attribute__((ext_vector_type(2)));
typedef u32 u32x4 __attribute__((ext_vector_type(4)));
typedef u32x4 rsrc_t;
typedef u32x4 bf16x8;
#define LAS __attribute__((address_space(3)))
#define DI __device__ __forceinline__
#define AI __attribute__((always_inline))
#define SBAR() __builtin_amdgcn_sched_barrier(0)

template <class F, int... Is> DI void sfor_(F&& f, std::integer_sequence<int, Is...>) { (f(std::integral_constant<int, Is>{}), ...); }
template <int N, class F> DI void sfor(F&& f) { sfor_(f, std::make_integer_sequence<int, N>{}); }

constexpr int SQ = 160, DM = 448, HQ = 16, HKV = 8, SK = 1024, DH = 256;
constexpr int ROWS = 80;
constexpr float SCL = 0.0625f * 1.4426950408889634f;
constexpr float PTHR = 1048576.0f;
constexpr float LTHR = 18.0f;

constexpr u32 L_VF = 0;
constexpr u32 L_KC = 65536;
constexpr u32 L_X = 90112;
constexpr u32 L_P = 131072;
constexpr u32 L_MX = 141312;
constexpr u32 L_FLAG = 143872;
constexpr u32 L_LS = L_KC + 6 * 8192;
constexpr u32 L_O = 122880;
constexpr u32 L_END = 163840;

#define KV_NT 3
#define KV_ORDER 0
template <bool NT = false> DI void dma(rsrc_t r, u32 voff, u32 soff, u32 ldsaddr) {
  if (NT) asm volatile("s_mov_b32 m0, %0\n\ts_nop 0\n\tbuffer_load_dwordx4 %1, %2, %3 offen nt lds" ::"s"(ldsaddr), "v"(voff), "s"(r), "s"(soff) : "memory");
  else asm volatile("s_mov_b32 m0, %0\n\ts_nop 0\n\tbuffer_load_dwordx4 %1, %2, %3 offen lds" ::"s"(ldsaddr), "v"(voff), "s"(r), "s"(soff) : "memory");
}
template <int LOFF> DI void set_m0(u32 lds) { asm volatile("s_add_u32 m0, %0, %1" ::"s"(lds), "i"(LOFF) : "memory", "scc"); }
template <int IMM, bool NT = false> DI void dma_m0(rsrc_t r, u32 voff, u32 soff) {
  if (NT) asm volatile("buffer_load_dwordx4 %0, %1, %2 offen offset:%3 nt lds" ::"v"(voff), "s"(r), "s"(soff), "i"(IMM) : "memory");
  else asm volatile("buffer_load_dwordx4 %0, %1, %2 offen offset:%3 lds" ::"v"(voff), "s"(r), "s"(soff), "i"(IMM) : "memory");
}
DI void mfma_acc(f32x4& c, const bf16x8& a, const bf16x8& b) { asm volatile("v_mfma_f32_16x16x32_bf16 %0, %1, %2, %0" : "+v"(c) : "v"(a), "v"(b)); }
DI void mfma_zero(f32x4& d, const bf16x8& a, const bf16x8& b) { asm volatile("v_mfma_f32_16x16x32_bf16 %0, %1, %2, 0" : "+v"(d) : "v"(a), "v"(b)); }
DI void pin4(const bf16x8 (&A)[4]) { asm volatile("" ::"v"(A[0]), "v"(A[1]), "v"(A[2]), "v"(A[3]) : "memory"); }
DI void pin5(const bf16x8 (&A)[5]) { asm volatile("" ::"v"(A[0]), "v"(A[1]), "v"(A[2]), "v"(A[3]), "v"(A[4]) : "memory"); }
template <int N> DI void wait_vm() { asm volatile("s_waitcnt vmcnt(%0)" ::"i"(N) : "memory"); }
DI void bar() { asm volatile("s_barrier" ::: "memory"); }
DI void bar_lds() { asm volatile("s_waitcnt lgkmcnt(0)\n\ts_barrier" ::: "memory"); }
DI bf16x8 ld128(u32 a) { return *(const LAS bf16x8*)a; }
DI f32x4 ldf4(u32 a) { return *(const LAS f32x4*)a; }
DI u32x4 ldu4(u32 a) { return *(const LAS u32x4*)a; }
DI u32x2 ldtr1(u32 a) { return __builtin_bit_cast(u32x2, __builtin_amdgcn_ds_read_tr16_b64_v4i16((LAS s16x4*)a)); }
DI u32 pk(float lo, float hi) { f32x2 v = {lo, hi}; return __builtin_bit_cast(u32, __builtin_convertvector(v, bf16x2)); }
DI f32x4 mfma(bf16x8 a, bf16x8 b, f32x4 c) { return __builtin_amdgcn_mfma_f32_16x16x32_bf16(__builtin_bit_cast(bf16x8_t, a), __builtin_bit_cast(bf16x8_t, b), c, 0, 0, 0); }
DI rsrc_t mkrsrc(const void* p, u32 bytes) { const uint64_t a = (uint64_t)p; return rsrc_t{(u32)a, (u32)(a >> 32) & 0xffffu, bytes, 0x00020000u}; }
DI u32 fresh_lane() { u32 l; asm volatile("v_mbcnt_lo_u32_b32 %0, -1, 0\n\tv_mbcnt_hi_u32_b32 %0, -1, %0" : "=v"(l)); return l; }
DI float ex2(float x) { return __builtin_amdgcn_exp2f(x); }
DI float max3(float a, float b, float c) { float r; asm("v_max3_f32 %0, %1, %2, %3" : "=v"(r) : "v"(a), "v"(b), "v"(c)); return r; }
DI float max2(float a, float b) { float r; asm("v_max_f32_e32 %0, %1, %2" : "=v"(r) : "v"(a), "v"(b)); return r; }
DI float fadd(float a, float b) { float r; asm("v_add_f32_e32 %0, %1, %2" : "=v"(r) : "v"(a), "v"(b)); return r; }
DI float xmax(float v) {
  auto a = __builtin_amdgcn_permlane16_swap(__float_as_uint(v), __float_as_uint(v), false, false);
  v = fmaxf(__uint_as_float(a[0]), __uint_as_float(a[1]));
  auto b = __builtin_amdgcn_permlane32_swap(__float_as_uint(v), __float_as_uint(v), false, false);
  return fmaxf(__uint_as_float(b[0]), __uint_as_float(b[1]));
}
DI float xsum(float v) {
  auto a = __builtin_amdgcn_permlane16_swap(__float_as_uint(v), __float_as_uint(v), false, false);
  v = __uint_as_float(a[0]) + __uint_as_float(a[1]);
  auto b = __builtin_amdgcn_permlane32_swap(__float_as_uint(v), __float_as_uint(v), false, false);
  return __uint_as_float(b[0]) + __uint_as_float(b[1]);
}
DI bf16x8 scale8(bf16x8 u, float f) {
#pragma unroll
  for (int i = 0; i < 4; ++i) {
    const float lo = __uint_as_float(u[i] << 16) * f, hi = __uint_as_float(u[i] & 0xffff0000u) * f;
    u32 r; asm("v_cvt_pk_bf16_f32 %0, %1, %2" : "=v"(r) : "v"(lo), "v"(hi));
    u[i] = r;
  }
  asm volatile("s_nop 1" : "+v"(u));
  return u;
}

#define ST(i)
#define STR(i)
#define ST_OUT()
#define NO_PV 0
#define NO_WR 0
#define KV_BYTES (SK * DH * 2)
#define NO_KDMA 0
#define VDMA 1

DI void readA1(bf16x8 (&A)[4], const u32 (&ab)[4], int e, int i) {
  const int dt = i >> 1;
  const u32x2 v = (i & 1) ? ldtr1(ab[dt ^ 2] + 4096 * e + 512) : ldtr1(ab[dt] + 4096 * e);
  if (i & 1) { A[dt][2] = v[0]; A[dt][3] = v[1]; } else { A[dt][0] = v[0]; A[dt][1] = v[1]; }
}
DI void readA(bf16x8 (&A)[4], const u32 (&ab)[4], int e) {
#pragma unroll
  for (int i = 0; i < 8; ++i) readA1(A, ab, e, i);
}
DI void readB(bf16x8 (&B)[5], u32 bb) {
#pragma unroll
  for (int qt = 0; qt < 5; ++qt) B[qt] = ld128(bb + qt * 2048);
}
constexpr int vpos[4] = {3, 8, 13, 18};
template <bool RF, bool LAST, int W1, int W0, bool NT = false, class Mid, class Ep>
DI void gstep(f32x4 (&acc)[4][5], bf16x8 (&A0)[4], bf16x8 (&B0)[5], bf16x8 (&B1)[5], u32 (&ab)[4], u32& slot, u32 b1,
              rsrc_t rr, u32 rsoff, const u32 (&rv)[8], Mid mid, Ep ep) {
  bf16x8 A1[4];
  sfor<20>([&](auto nc) AI {
    constexpr int n = nc.value, j = n == vpos[0] ? 0 : n == vpos[1] ? 1 : n == vpos[2] ? 2 : n == vpos[3] ? 3 : -1;
    if constexpr (RF && j >= 0) { if (j == 0) pin4(A0); set_m0<1024 * (j < 0 ? 0 : j)>(slot); SBAR(); }
    acc[n / 5][n % 5] = mfma(A0[n / 5], B0[n % 5], acc[n / 5][n % 5]);
    SBAR();
    if constexpr (RF && j >= 0 && VDMA) dma_m0<0, NT>(rr, rv[j < 0 ? 0 : j], rsoff);
    if constexpr (RF && VDMA == 2 && n % 5 == 0 && n > 0) dma_m0<0>(rr, rv[n / 5 - 1], rsoff);
    if (n < 5) B1[n % 5] = ld128(b1 + n * 2048);
    if (n == 5) wait_vm<W1>();
    if (n >= 5 && n < 13) readA1(A1, ab, 1, n - 5);
    if (n == 16) { ab[0] ^= 8192u; ab[1] ^= 8192u; asm volatile("" : "+v"(ab[0]), "+v"(ab[1])); }
    if (n == 17) { ab[2] ^= 8192u; ab[3] ^= 8192u; asm volatile("" : "+v"(ab[2]), "+v"(ab[3])); }
    SBAR();
  });
  const u32 b0 = mid();
  SBAR();
  sfor<20>([&](auto nc) AI {
    constexpr int n = nc.value, j = n == vpos[0] ? 4 : n == vpos[1] ? 5 : n == vpos[2] ? 6 : n == vpos[3] ? 7 : -1;
    constexpr int dt = LAST ? n % 4 : n / 5, qt = LAST ? n / 4 : n % 5;
    if constexpr (RF && j >= 0) { if (j == 4) pin4(A1); set_m0<1024 * (j < 0 ? 0 : j)>(slot); SBAR(); }
    acc[dt][qt] = mfma(A1[dt], B1[qt], acc[dt][qt]);
    SBAR();
    if constexpr (RF && j >= 0 && VDMA) dma_m0<0, NT>(rr, rv[j < 0 ? 0 : j], rsoff);
    if constexpr (RF && VDMA == 2 && n % 5 == 0) dma_m0<0>(rr, rv[n / 5 + 3], rsoff);
    if constexpr (RF && VDMA == 2 && n == 19) dma_m0<0>(rr, rv[7], rsoff);
    if constexpr (!LAST) {
      if (n < 5) B0[n % 5] = ld128(b0 + n * 2048);
      if (n == 5) wait_vm<W0>();
      if (n >= 5 && n < 13) readA1(A0, ab, 0, n - 5);
    } else if constexpr (n >= 2) ep(std::integral_constant<int, (n - 2) % 4>{}, std::integral_constant<int, (n - 2) / 4>{});
    SBAR();
  });
  if constexpr (LAST) { ep(std::integral_constant<int, 2>{}, std::integral_constant<int, 4>{}); ep(std::integral_constant<int, 3>{}, std::integral_constant<int, 4>{}); }
  slot ^= 8192u;
}
struct NoEp { template <class A, class B> DI void operator()(A, B) const {} };

constexpr int kpos[8] = {6, 11, 16, 21, 26, 31, 36, 38};
constexpr int kcount(int n) { int c = 0; for (int j = 0; j < 8; ++j) c += kpos[j] < n; return c; }
constexpr u32 kv_tile(u32 cu, u32 s) { if (KV_ORDER == 0) return s & 15; const u32 d = (cu - s) & 3; const u32 g = d == 0 ? 0 : (d == 1 ? 2 : 3); return (s - g) & 15; }
constexpr uint64_t kv_seq(u32 cu) { uint64_t t = 0; for (u32 i = 0; i < 16; ++i) t |= (uint64_t)kv_tile(cu, i) << (4 * i); return t; }

__global__ __launch_bounds__(512, 2) void fused_attn(const u16* __restrict__ x, const u16* __restrict__ kc, const u16* __restrict__ vc,
                                                     const u16* __restrict__ wq, const u16* __restrict__ wo, u32x4* __restrict__ part) {
  const u32 lane = threadIdx.x & 63, wid = __builtin_amdgcn_readfirstlane(threadIdx.x >> 6), w = wid & 3;
  const u32 blk = blockIdx.x, kvh = blk & 7, idx = blk >> 3, b = idx >> 2, cu = idx & 3, head = 2 * kvh + (cu >> 1), half = cu & 1;
  ST(0); STR(40);
  const u32 q = lane & 15, g = lane >> 4;
  const u32 lr = lane >> 3, lc = (lane & 7) ^ (lr & 6);
  const u32 rot = 2 * b + half, rot7 = rot % 7;
  const uint64_t tseq = cu == 0 ? kv_seq(0) : cu == 1 ? kv_seq(1) : cu == 2 ? kv_seq(2) : kv_seq(3);
  auto tile_off = [&](u32 s) AI { return ((u32)(tseq >> (4 * s)) & 15) * (64 * 512); };
  const u32 nb = wid < 4 ? w : 3 + w;
  auto wo_soff = [&](u32 j) AI { return ((rot + j) & 3) * 64 * (DM * 2) + nb * 128; };

  f32x4 acc[4][5];
  bf16x8 A0[4], B0[5], B1[5];
  u32 ab[4], rv[8], slot = wid < 4 ? L_KC + 16384 * w : L_VF + 16384 * w;
  auto init_ab = [&]() AI {
    const u32 lane = fresh_lane(), g = lane >> 4;
    const u32 q4 = (lane & 15) >> 2, p = lane & 3, f0 = (q4 & 2) ^ ((g & 1) << 2);
#pragma unroll
    for (int i = 0; i < 4; ++i) ab[i] = slot + g * 1024 + q4 * 128 + 16 * (((2 * i) ^ f0) | (p >> 1)) + 8 * (p & 1);
  };
  auto init_rv = [&](u32 stride) AI {
    const u32 lane = fresh_lane(), lr = lane >> 3, lc = (lane & 7) ^ (lr & 6);
#pragma unroll
    for (int i = 0; i < 8; ++i) rv[i] = (lr + 8 * i) * stride + ((lc * 16) ^ ((i & 1) * 64));
  };
  auto issue = [&](rsrc_t rr, u32 so, u32 dst) AI {
#pragma unroll
    for (int i = 0; i < 8; ++i) dma(rr, rv[i], so, dst + i * 1024);
  };
  auto zero_acc = [&]() AI {
#pragma unroll
    for (int dt = 0; dt < 4; ++dt)
#pragma unroll
      for (int qt = 0; qt < 5; ++qt) acc[dt][qt] = f32x4{0.f, 0.f, 0.f, 0.f};
  };
  const rsrc_t rWo = mkrsrc(wo + (size_t)head * DH * DM, DH * DM * 2);

  if (wid < 4) {
    {
      const rsrc_t rX = mkrsrc(x + (size_t)(b * SQ + half * ROWS) * DM, ROWS * DM * 2);
      const u32 voffX = lr * (DM * 2) + lc * 16;
      u32 v = rot7, dst = L_X;
#pragma unroll 1
      for (u32 vp = 0; vp < 7; ++vp) {
        dma(rX, voffX, w * 8 * (DM * 2) + v * 128, dst + w * 1024);
        dma(rX, voffX, (w + 4) * 8 * (DM * 2) + v * 128, dst + (w + 4) * 1024);
        if (w < 2) { dma(rX, voffX, (w + 8) * 8 * (DM * 2) + v * 128, dst + (w + 8) * 1024); wait_vm<3>(); } else wait_vm<2>();
        if (vp > 0) bar();
        v = v == 6 ? 0 : v + 1; dst += 10240;
      }
    }
    const rsrc_t rK = mkrsrc(kc + (size_t)(b * HKV + kvh) * SK * DH, KV_BYTES);
    const u32 vK0 = lr * 512 + lc * 16, vK1 = vK0 + 4096;
    ST(1);
    wait_vm<0>();
    bar();
    ST(2);
#pragma unroll 1
    for (u32 j = 0; j < 2; ++j) {
      const u32 so = tile_off(j) + 16 * w * 512, ks = slot + j * 8192;
#pragma unroll
      for (int v = 0; v < 4; ++v) { dma<(KV_NT & 1) != 0>(rK, vK0, so + 128 * v, ks + v * 1024); dma<(KV_NT & 1) != 0>(rK, vK1, so + 128 * v, ks + 4096 + v * 1024); }
    }
    u32 kb0 = slot + (q >> 3) * 4096 + (q & 7) * 128 + 16 * (g ^ (q & 6)), kb1 = kb0 ^ 64;
    u32 pw = L_P + (((2 * w + (g >> 1)) & 3) * 16 + q) * 16 + (g & 1) * 8 + (w >> 1) * 1024;
    float mref[5], l[5];
#pragma unroll
    for (int qt = 0; qt < 5; ++qt) { mref[qt] = 0.f; l[qt] = 0.f; }
    f32x4 S[2][5];
    bf16x8 kf[4];
    auto rdK = [&](int u) AI { return ld128(((u & 1) ? kb1 : kb0) + (u >> 1) * 1024); };
    ST(5);
    bar();
    ST(6);
    bf16x8 Q[8][5];
    auto rdQ = [&](int u, int qt) AI { Q[u][qt] = ld128(lane * 16 + (qt < 4 ? L_VF + 16384 * (u >> 1) + qt * 2048 : L_P + 2048 * (u >> 1)) + (u & 1) * 1024); };
    wait_vm<8>();
    sfor<8>([&](auto uc) AI {
      constexpr int u = uc.value;
      if (u == 0) { kf[0] = rdK(0); rdQ(0, 0); rdQ(0, 1); rdQ(0, 2); rdQ(0, 3); rdQ(0, 4); }
      if (u < 7) { kf[(u + 1) & 1] = rdK(u + 1); rdQ((u + 1) & 7, 0); rdQ((u + 1) & 7, 1); rdQ((u + 1) & 7, 2); rdQ((u + 1) & 7, 3); rdQ((u + 1) & 7, 4); }
#pragma unroll
      for (int qt = 0; qt < 5; ++qt) S[0][qt] = mfma(kf[u & 1], Q[u][qt], u == 0 ? f32x4{0.f, 0.f, 0.f, 0.f} : S[0][qt]);
      SBAR();
    });
#pragma unroll
    for (int qt = 0; qt < 5; ++qt) S[1][qt] = f32x4{0.f, 0.f, 0.f, 0.f};
    u32 fla = L_FLAG + 16 + (lane & 3) * 4;
    const u32 flw = (w - (lane & 3)) * 4;
    *(LAS u32*)(fla + flw) = 0;
    bar_lds();
    {
      const u32 so = tile_off(2) + 16 * w * 512;
#pragma unroll
      for (int v = 0; v < 4; ++v) { dma<(KV_NT & 1) != 0>(rK, vK0, so + 128 * v, slot + v * 1024); dma<(KV_NT & 1) != 0>(rK, vK1, so + 128 * v, slot + 4096 + v * 1024); }
    }
    kb0 ^= 8192u; kb1 ^= 8192u; slot ^= 8192u;
    wait_vm<12>();
    kf[0] = rdK(0); kf[1] = rdK(1); kf[2] = rdK(2);
    ST(7);

    auto adopt = [&](u32 par, bool s0) AI {
      const u32x4 f4 = ldu4(L_FLAG + par * 16);
      const u32 q = fresh_lane() & 15;
#pragma unroll
      for (int qt = 0; qt < 5; ++qt) {
        const f32x4 m4 = ldf4(L_MX + par * 1280 + (qt * 16 + q) * 16);
        const float own = mref[qt], base = s0 ? 0.f : own;
        float M = own;
#pragma unroll
        for (int i = 0; i < 4; ++i) M = fmaxf(M, f4[i] ? m4[i] : base);
        l[qt] *= ex2(own - M); mref[qt] = M;
        asm volatile("" : "+v"(l[qt]), "+v"(mref[qt]));
        SBAR();
      }
    };
    float t[4], ps[5];
    u32x2 pkd;
    float thr = -1.f;
    const u32 zero = 0;
    auto step = [&](auto xc, auto mc, u32 s) AI {
      constexpr int X = xc.value; constexpr bool MM = mc.value;
      u32 fl, so; float chk; uint64_t mask;
      sfor<40>([&](auto nc) AI {
        constexpr int n = nc.value, u = n / 5, qt = n % 5;
        constexpr bool kd = MM && kcount(n + 1) != kcount(n) && !NO_KDMA;
        constexpr int j = kcount(n), jv = j >> 1, jh = j & 1;
        if constexpr (kd) set_m0<jh * 4096 + jv * (1024 - 128)>(slot);
        if constexpr (MM) { if (u == 0) mfma_zero(S[X ^ 1][qt], kf[u & 3], Q[u][qt]); else mfma_acc(S[X ^ 1][qt], kf[u & 3], Q[u][qt]); }
        if constexpr (kd) dma_m0<128 * jv, (KV_NT & 1) != 0>(rK, jh ? vK1 : vK0, so);
        SBAR();
        if constexpr (MM && qt == 0) {
          if constexpr (u == 1 || u == 3) wait_vm<8 + 2 * (3 - (u + 3) / 2) + kcount(n + 1)>();
          if constexpr (u == 5 || u == 7) wait_vm<2 * (3 - (u - 5) / 2) + kcount(n + 1)>();
          kf[(u + 3) & 3] = rdK((u + 3) & 7);
        }
        if (n == 1) fl = *(const LAS u32*)fla;
        if (MM && n == 2) {
          so = (s < 13 ? tile_off((s + 3) & 15) : (u32)(SK * DH * 2)) + 16 * w * 512;
          asm volatile("" : "+s"(so));
        }
        if (MM && n == 22) { kb0 ^= 8192u; kb1 ^= 8192u; }
        if constexpr (n >= 1 && n < 31) {
          constexpr int c = (n - 1) / 6, k = (n - 1) % 6;
          if (k == 0) { t[0] = fmaf(S[X][c][0], SCL, -mref[c]); t[1] = fmaf(S[X][c][1], SCL, -mref[c]); asm volatile("" : "+v"(t[0])); asm volatile("" : "+v"(t[1])); }
          if (k == 1) { t[2] = fmaf(S[X][c][2], SCL, -mref[c]); t[0] = ex2(t[0]); t[3] = fmaf(S[X][c][3], SCL, -mref[c]); asm volatile("" : "+v"(t[2])); asm volatile("" : "+v"(t[3])); }
          if (k == 2) { t[1] = ex2(t[1]); t[2] = ex2(t[2]); }
          if (k == 3) { t[3] = ex2(t[3]); pkd[0] = pk(t[0], t[1]); t[0] = fadd(t[0], t[1]); }
          if (k == 4) { pkd[1] = pk(t[2], t[3]); t[2] = fadd(t[2], t[3]); }
          if (k == 5) { if (NO_WR) asm volatile("" :: "v"(pkd)); else *(LAS u32x2*)(pw + c * 2048) = pkd; ps[c] = fadd(t[0], t[2]); }
        }
        if (n == 26) { asm volatile("" : "+v"(fl)); chk = max3(ps[0], ps[1], __uint_as_float(fl)); fla ^= 16u; }
        if (n == 28) { chk = max3(chk, ps[2], ps[3]); if (!NO_WR || s < 2) *(LAS u32*)(fla + flw) = zero; }
        if (n == 31) chk = max2(chk, ps[4]);
        if (n == 32) { mask = __builtin_amdgcn_ballot_w64(chk > thr); asm volatile("" : "+s"(mask)); }
        if (n == 33) {
          if (__builtin_expect(mask != 0, 0)) {
            const bool ad = __any(fl != 0), s0 = s == 0;
            if (ad) adopt((s + 1) & 1, s == 1);
            float mm = -__builtin_inff(), mn = __builtin_inff();
#pragma unroll
            for (int c = 0; c < 5; ++c) {
              const float m = fmaf(fmaxf(fmaxf(S[X][c][0], S[X][c][1]), fmaxf(S[X][c][2], S[X][c][3])), SCL, -mref[c]);
              mm = fmaxf(mm, m); mn = fminf(mn, m);
            }
            const bool move = __any(mm > LTHR || (s0 && mn < -LTHR));
            if (move || ad) {
              const u32 q = fresh_lane() & 15;
#pragma unroll
              for (int c = 0; c < 5; ++c) {
                float d = __builtin_ceilf(xmax(fmaf(fmaxf(fmaxf(S[X][c][0], S[X][c][1]), fmaxf(S[X][c][2], S[X][c][3])), SCL, -mref[c])));
                d = move ? (s0 ? d : fmaxf(d, 0.f)) : 0.f;
                l[c] *= ex2(fminf(-d, 0.f)); mref[c] += d;
                const float p0 = ex2(fmaf(S[X][c][0], SCL, -mref[c])), p1 = ex2(fmaf(S[X][c][1], SCL, -mref[c]));
                const float p2 = ex2(fmaf(S[X][c][2], SCL, -mref[c])), p3 = ex2(fmaf(S[X][c][3], SCL, -mref[c]));
                *(LAS u32x2*)(pw + c * 2048) = u32x2{pk(p0, p1), pk(p2, p3)};
                ps[c] = (p0 + p1) + (p2 + p3);
                *(LAS float*)(L_MX + (s & 1) * 1280 + (c * 16 + q) * 16 + w * 4) = mref[c];
                asm volatile("" : "+v"(ps[c]), "+v"(l[c]), "+v"(mref[c]));
                SBAR();
              }
              if (move) *(LAS u32*)(fla + flw) = 0x7f800000u;
            }
            thr = PTHR;
          }
        }
        if (n == 34) { l[0] = fadd(l[0], ps[0]); l[1] = fadd(l[1], ps[1]); }
        if (n == 36) { l[2] = fadd(l[2], ps[2]); l[3] = fadd(l[3], ps[3]); }
        if (n == 37) { l[4] = fadd(l[4], ps[4]); pw ^= 16384u; asm volatile("" : "+v"(pw)); }
        if (MM && n == 39) { slot ^= 8192u; asm volatile("" : "+s"(slot)); }
        SBAR();
      });
      if constexpr (MM) asm volatile("s_waitcnt lgkmcnt(1)\n\ts_barrier" ::: "memory");
      else bar_lds();
    };
    using I0 = std::integral_constant<int, 0>; using I1 = std::integral_constant<int, 1>;
    step(I0{}, I1{}, 0);
    ST(9); STR(41);
#pragma unroll 1
    for (u32 s = 1; s < 15; s += 2) {
      step(I1{}, I1{}, s); step(I0{}, I1{}, s + 1);
    }
    ST(10); STR(42);
    wait_vm<0>();
    asm volatile("s_nop 7" ::: "memory");
    step(I1{}, I0{}, 15);
    slot = L_KC + 16384 * w;
    if (__any(*(const LAS u32*)fla != 0)) adopt(1, false);
    { const u32 q = fresh_lane() & 15;
#pragma unroll
      for (int qt = 0; qt < 5; ++qt) *(LAS float*)(L_LS + (qt * 16 + q) * 16 + w * 4) = xsum(l[qt]);
    }
    init_rv(DM * 2);
    if (w < 3) { issue(rWo, wo_soff(0), slot); issue(rWo, wo_soff(1), slot + 8192); }
    bar_lds();
    init_ab();
    wait_vm<12>();
    readA(A0, ab, 0);
    zero_acc();
    ST(58);
    bar();
    ST(59);
  } else {
    const rsrc_t rWq = mkrsrc(wq + head * DH, (DM * HQ * DH - head * DH) * 2);
    auto wq_soff = [&](u32 vp) AI { u32 v = vp + rot7; v = v >= 7 ? v - 7 : v; return v * 64 * (HQ * DH * 2) + w * 128; };
    init_rv(HQ * DH * 2);
    issue(rWq, wq_soff(0), slot);
    init_ab();
    zero_acc();
    SBAR();
    issue(rWq, wq_soff(1), slot + 8192);
    const rsrc_t rV = mkrsrc(vc + (size_t)(b * HKV + kvh) * SK * DH, KV_BYTES);
    const u32 wa = ((g >> 1) * 16 + q) * 16 + (g & 1) * 8;
    wait_vm<12>();
    readA(A0, ab, 0);
    {
      u32 xb = L_X + (q >> 3) * 1024 + (q & 7) * 128 + 16 * (g ^ (q & 6));
      bar();
      ST(1);
      readB(B0, xb);
      auto mid = [&]() AI { pin5(B1); bar();   xb += 10240; return xb; };
#pragma unroll 1
      for (u32 vp = 0; vp < 5; ++vp) gstep<true, false, 9, 9>(acc, A0, B0, B1, ab, slot, xb ^ 64, rWq, wq_soff(vp + 2), rv, mid, NoEp{});
      ST(2);
      gstep<false, false, 8, 4>(acc, A0, B0, B1, ab, slot, xb ^ 64, rWq, 0, rv, mid, NoEp{});
      ST(3);
      const u32 qa = slot + wa, qa2 = L_P + 2048 * w + wa;
      gstep<false, true, 0, 0>(acc, A0, B0, B1, ab, slot, xb ^ 64, rWq, 0, rv, [&]() AI { return 0u; }, [&](auto dtc, auto qtc) AI {
        constexpr int dt = dtc.value, qt = qtc.value;
        const f32x4 v = acc[dt][qt];
        *(LAS u32x2*)((qt < 4 ? qa + qt * 2048 : qa2) + (dt >> 1) * 1024 + (dt & 1) * 512) = u32x2{pk(v[0], v[1]), pk(v[2], v[3])};
      });
      ST(4);
      bar_lds();
      init_rv(512);
      issue(rV, tile_off(0) + w * 128, slot);
      zero_acc();
      bar();
      issue(rV, tile_off(1) + w * 128, slot ^ 8192u);
      wait_vm<12>();
      readA(A0, ab, 0);
      ST(5);
    }
    float mref[5];
#pragma unroll
    for (int qt = 0; qt < 5; ++qt) mref[qt] = 0.f;
    u32 pb = L_P + lane * 16;
    u32 fla = L_FLAG + (lane & 3) * 4;
    bar();
    ST(6);
    u32 fl = *(const LAS u32*)fla;
    readB(B0, pb);
    rsrc_t rr = rV;
    float f1[5];
    bool rare;
    auto pre = [&](u32 s) AI {
      rare = __any(fl != 0);
      if (__builtin_expect(rare, 0)) {
        const u32x4 f4 = ldu4(L_FLAG + (s & 1) * 16);
#pragma unroll
        for (int qt = 0; qt < 5; ++qt) {
          const f32x4 m4 = ldf4(L_MX + (s & 1) * 1280 + (qt * 16 + q) * 16);
          float r[4];
#pragma unroll
          for (int i = 0; i < 4; ++i) r[i] = f4[i] ? m4[i] : mref[qt];
          const float M = fmaxf(fmaxf(r[0], r[1]), fmaxf(r[2], r[3]));
          const float f = ex2(fminf(mref[qt] - M, 0.f));
#pragma unroll
          for (int dt = 0; dt < 4; ++dt) acc[dt][qt] *= f;
          B0[qt] = scale8(B0[qt], ex2(((g >> 1) ? r[1] : r[0]) - M));
          f1[qt] = ex2(((g >> 1) ? r[3] : r[2]) - M);
          mref[qt] = M;
        }
      }
    };
    auto mid = [&]() AI {
      pb ^= 16384u; fla ^= 16u;
      pin5(B1);
      if (__builtin_expect(rare, 0)) {
#pragma unroll
        for (int qt = 0; qt < 5; ++qt) B1[qt] = scale8(B1[qt], f1[qt]);
      }
      bar();
    };
#pragma unroll 1
    for (u32 s = 0; s < 15; ++s) {
      if (NO_PV) { bar(); continue; }
      u32 rsoff = tile_off((s + 2) & 15) + w * 128;
      if (__builtin_expect(s == 14, 0)) { rr = rWo; rsoff = wo_soff(0); init_rv(DM * 2); }
      pre(s);
      gstep<true, false, 9, 9, (KV_NT & 2) != 0>(acc, A0, B0, B1, ab, slot, pb + 1024, rr, rsoff, rv, [&]() AI { mid(); fl = *(const LAS u32*)fla; return pb; }, NoEp{});
    }
    ST(56);
    pre(15);
    float inv[5];
    const u32 oa = L_O + w * 10240 + wa;
    gstep<true, true, 9, 0>(acc, A0, B0, B1, ab, slot, pb + 1024, rWo, wo_soff(1), rv, [&]() AI {
      mid();
#pragma unroll
      for (int qt = 0; qt < 5; ++qt) { const f32x4 l4 = ldf4(L_LS + (qt * 16 + q) * 16); inv[qt] = __builtin_amdgcn_rcpf((l4[0] + l4[1]) + (l4[2] + l4[3])); }
      return 0u;
    }, [&](auto dtc, auto qtc) AI {
      constexpr int dt = dtc.value, qt = qtc.value;
      const f32x4 v = acc[dt][qt] * inv[qt];
      *(LAS u32x2*)(oa + qt * 2048 + (dt >> 1) * 1024 + (dt & 1) * 512) = u32x2{pk(v[0], v[1]), pk(v[2], v[3])};
    });
    wait_vm<12>();
    readA(A0, ab, 0);
    zero_acc();
    ST(58);
    asm volatile("s_waitcnt lgkmcnt(8)\n\ts_barrier" ::: "memory");
    ST(59);
  }
  if (wid != 3) {
    const u32 lane = fresh_lane();
    slot = wid < 4 ? L_KC + 16384 * w : L_VF + 16384 * w + 8192;
    auto ob = [&](u32 j) AI { return L_O + ((rot + j) & 3) * 10240 + lane * 16; };
    readB(B0, ob(0));
#pragma unroll 1
    for (u32 j = 0; j < 2; ++j)
      gstep<true, false, 9, 9>(acc, A0, B0, B1, ab, slot, ob(j) + 1024, rWo, wo_soff(j + 2), rv, [&]() AI { return ob(j + 1); }, NoEp{});
    gstep<false, false, 8, 4>(acc, A0, B0, B1, ab, slot, ob(2) + 1024, rWo, 0, rv, [&]() AI { return ob(3); }, NoEp{});
    ST(60);
    u32x4* dst = part + ((size_t)((2 * b + half) * HQ + head) * 70 + nb * 10) * 64 + lane;
    gstep<false, true, 0, 0>(acc, A0, B0, B1, ab, slot, ob(3) + 1024, rWo, 0, rv, [&]() AI { return 0u; }, [&](auto dtc, auto qtc) AI {
      constexpr int dt = dtc.value, qt = qtc.value;
      if constexpr (dt & 1) {
        const f32x4 a = acc[dt - 1][qt], c = acc[dt][qt];
        dst[(qt * 2 + (dt >> 1)) * 64] = u32x4{pk(a[0], a[1]), pk(a[2], a[3]), pk(c[0], c[1]), pk(c[2], c[3])};
      }
    });
  }
  ST_OUT();
}

__global__ __launch_bounds__(256) void reduce_heads(const u32x4* __restrict__ part, u16* __restrict__ out) {
  const u32 t = blockIdx.x * 256 + threadIdx.x;
  const u32 lane = t & 63, unit = (t >> 6) % 70, grp = (t >> 6) / 70;
  const u32x4* src = part + ((size_t)grp * HQ * 70 + unit) * 64 + lane;
  float a[8] = {0.f, 0.f, 0.f, 0.f, 0.f, 0.f, 0.f, 0.f};
  u32x4 v[16];
#pragma unroll
  for (int h = 0; h < 16; ++h) v[h] = src[(size_t)h * 70 * 64];
#pragma unroll
  for (int h = 0; h < 16; ++h)
#pragma unroll
    for (int i = 0; i < 4; ++i) { a[2 * i] += __uint_as_float(v[h][i] << 16); a[2 * i + 1] += __uint_as_float(v[h][i] & 0xffff0000u); }
  const u32 nb = unit / 10, r = unit % 10, qt = r >> 1, np = r & 1, q = lane & 15, g = lane >> 4;
  u16* o = out + (size_t)(grp * ROWS + qt * 16 + q) * DM + nb * 64 + np * 32 + 4 * g;
  *(u32x2*)o = u32x2{pk(a[0], a[1]), pk(a[2], a[3])};
  *(u32x2*)(o + 16) = u32x2{pk(a[4], a[5]), pk(a[6], a[7])};
}

extern "C" void kernel_launch(void* const* d_in, const int* in_sizes, int n_in, void* d_out, int out_size, void* d_ws, size_t ws_size,
                              hipStream_t stream) {
  (void)in_sizes; (void)n_in; (void)out_size; (void)ws_size;
  (void)hipFuncSetAttribute((const void*)fused_attn, hipFuncAttributeMaxDynamicSharedMemorySize, (int)L_END);
  hipLaunchKernelGGL(fused_attn, dim3(256), dim3(512), L_END, stream, (const u16*)d_in[0], (const u16*)d_in[1], (const u16*)d_in[2],
                     (const u16*)d_in[3], (const u16*)d_in[4], (u32x4*)d_ws);
  hipLaunchKernelGGL(reduce_heads, dim3(280), dim3(256), 0, stream, (const u32x4*)d_ws, (u16*)d_out);
}
```

```cpp
#include <hip/hip_runtime.h>
#include <stdint.h>
#include <utility>

typedef unsigned int u32;
typedef unsigned short u16;
typedef __bf16 bf16x8_t __attribute__((ext_vector_type(8)));
typedef __bf16 bf16x2 __attribute__((ext_vector_type(2)));
typedef short s16x4 __attribute__((ext_vector_type(4)));
typedef float f32x4 __attribute__((ext_vector_type(4)));
typedef float f32x2 __attribute__((ext_vector_type(2)));
typedef u32 u32x2 __attribute__((ext_vector_type(2)));
typedef u32 u32x4 __attribute__((ext_vector_type(4)));
typedef u32x4 rsrc_t;
typedef u32x4 bf16x8;
#define LAS __attribute__((address_space(3)))
#define DI __device__ __forceinline__
#define AI __attribute__((always_inline))
#define SBAR() __builtin_amdgcn_sched_barrier(0)

template <class F, int... Is> DI void sfor_(F&& f, std::integer_sequence<int, Is...>) { (f(std::integral_constant<int, Is>{}), ...); }
template <int N, class F> DI void sfor(F&& f) { sfor_(f, std::make_integer_sequence<int, N>{}); }

constexpr int SQ = 160, DM = 448, HQ = 16, HKV = 8, SK = 1024, DH = 256;
constexpr int ROWS = 80;
constexpr float SCL = 0.0625f * 1.4426950408889634f;
constexpr float PTHR = 1048576.0f;
constexpr float LTHR = 18.0f;

constexpr u32 L_VF = 0;
constexpr u32 L_KC = 65536;
constexpr u32 L_X = 90112;
constexpr u32 L_P = 131072;
constexpr u32 L_MX = 141312;
constexpr u32 L_FLAG = 143872;
constexpr u32 L_LS = L_KC + 6 * 8192;
constexpr u32 L_O = 122880;
constexpr u32 L_END = 163840;
constexpr u32 W_EPOCH = 0, W_FLAG = 16 * 128, W_PART = 65536, PART_BYTES = 70 * 1024;
typedef __attribute__((address_space(1))) unsigned long long gu64;

#define KV_NT 0
#define KV_ORDER 1
template <bool NT = false> DI void dma(rsrc_t r, u32 voff, u32 soff, u32 ldsaddr) {
  if (NT) asm volatile("s_mov_b32 m0, %0\n\ts_nop 0\n\tbuffer_load_dwordx4 %1, %2, %3 offen nt lds" ::"s"(ldsaddr), "v"(voff), "s"(r), "s"(soff) : "memory");
  else asm volatile("s_mov_b32 m0, %0\n\ts_nop 0\n\tbuffer_load_dwordx4 %1, %2, %3 offen lds" ::"s"(ldsaddr), "v"(voff), "s"(r), "s"(soff) : "memory");
}
template <int LOFF> DI void set_m0(u32 lds) { asm volatile("s_add_u32 m0, %0, %1" ::"s"(lds), "i"(LOFF) : "memory", "scc"); }
template <int IMM, bool NT = false> DI void dma_m0(rsrc_t r, u32 voff, u32 soff) {
  if (NT) asm volatile("buffer_load_dwordx4 %0, %1, %2 offen offset:%3 nt lds" ::"v"(voff), "s"(r), "s"(soff), "i"(IMM) : "memory");
  else asm volatile("buffer_load_dwordx4 %0, %1, %2 offen offset:%3 lds" ::"v"(voff), "s"(r), "s"(soff), "i"(IMM) : "memory");
}
DI void mfma_acc(f32x4& c, const bf16x8& a, const bf16x8& b) { asm volatile("v_mfma_f32_16x16x32_bf16 %0, %1, %2, %0" : "+v"(c) : "v"(a), "v"(b)); }
DI void mfma_zero(f32x4& d, const bf16x8& a, const bf16x8& b) { asm volatile("v_mfma_f32_16x16x32_bf16 %0, %1, %2, 0" : "+v"(d) : "v"(a), "v"(b)); }
DI void pin4(const bf16x8 (&A)[4]) { asm volatile("" ::"v"(A[0]), "v"(A[1]), "v"(A[2]), "v"(A[3]) : "memory"); }
DI void pin5(const bf16x8 (&A)[5]) { asm volatile("" ::"v"(A[0]), "v"(A[1]), "v"(A[2]), "v"(A[3]), "v"(A[4]) : "memory"); }
template <int N> DI void wait_vm() { asm volatile("s_waitcnt vmcnt(%0)" ::"i"(N) : "memory"); }
DI void bar() { asm volatile("s_barrier" ::: "memory"); }
DI void bar_lds() { asm volatile("s_waitcnt lgkmcnt(0)\n\ts_barrier" ::: "memory"); }
DI bf16x8 ld128(u32 a) { return *(const LAS bf16x8*)a; }
DI f32x4 ldf4(u32 a) { return *(const LAS f32x4*)a; }
DI u32x4 ldu4(u32 a) { return *(const LAS u32x4*)a; }
DI u32x2 ldtr1(u32 a) { return __builtin_bit_cast(u32x2, __builtin_amdgcn_ds_read_tr16_b64_v4i16((LAS s16x4*)a)); }
DI u32 pk(float lo, float hi) { f32x2 v = {lo, hi}; return __builtin_bit_cast(u32, __builtin_convertvector(v, bf16x2)); }
DI f32x4 mfma(bf16x8 a, bf16x8 b, f32x4 c) { return __builtin_amdgcn_mfma_f32_16x16x32_bf16(__builtin_bit_cast(bf16x8_t, a), __builtin_bit_cast(bf16x8_t, b), c, 0, 0, 0); }
DI rsrc_t mkrsrc(const void* p, u32 bytes) { const uint64_t a = (uint64_t)p; return rsrc_t{(u32)a, (u32)(a >> 32) & 0xffffu, bytes, 0x00020000u}; }
DI u32 fresh_lane() { u32 l; asm volatile("v_mbcnt_lo_u32_b32 %0, -1, 0\n\tv_mbcnt_hi_u32_b32 %0, -1, %0" : "=v"(l)); return l; }
DI float ex2(float x) { return __builtin_amdgcn_exp2f(x); }
DI float max3(float a, float b, float c) { float r; asm("v_max3_f32 %0, %1, %2, %3" : "=v"(r) : "v"(a), "v"(b), "v"(c)); return r; }
DI float max2(float a, float b) { float r; asm("v_max_f32_e32 %0, %1, %2" : "=v"(r) : "v"(a), "v"(b)); return r; }
DI float fadd(float a, float b) { float r; asm("v_add_f32_e32 %0, %1, %2" : "=v"(r) : "v"(a), "v"(b)); return r; }
DI float xmax(float v) {
  auto a = __builtin_amdgcn_permlane16_swap(__float_as_uint(v), __float_as_uint(v), false, false);
  v = fmaxf(__uint_as_float(a[0]), __uint_as_float(a[1]));
  auto b = __builtin_amdgcn_permlane32_swap(__float_as_uint(v), __float_as_uint(v), false, false);
  return fmaxf(__uint_as_float(b[0]), __uint_as_float(b[1]));
}
DI float xsum(float v) {
  auto a = __builtin_amdgcn_permlane16_swap(__float_as_uint(v), __float_as_uint(v), false, false);
  v = __uint_as_float(a[0]) + __uint_as_float(a[1]);
  auto b = __builtin_amdgcn_permlane32_swap(__float_as_uint(v), __float_as_uint(v), false, false);
  return __uint_as_float(b[0]) + __uint_as_float(b[1]);
}
DI bf16x8 scale8(bf16x8 u, float f) {
#pragma unroll
  for (int i = 0; i < 4; ++i) {
    const float lo = __uint_as_float(u[i] << 16) * f, hi = __uint_as_float(u[i] & 0xffff0000u) * f;
    u32 r; asm("v_cvt_pk_bf16_f32 %0, %1, %2" : "=v"(r) : "v"(lo), "v"(hi));
    u[i] = r;
  }
  asm volatile("s_nop 1" : "+v"(u));
  return u;
}

#define ST(i)
#define STR(i)
#define ST_OUT()
#define NO_PV 0
#define NO_WR 0
#define KV_BYTES (SK * DH * 2)
#define NO_KDMA 0
#define VDMA 1

DI void readA1(bf16x8 (&A)[4], const u32 (&ab)[4], int e, int i) {
  const int dt = i >> 1;
  const u32x2 v = (i & 1) ? ldtr1(ab[dt ^ 2] + 4096 * e + 512) : ldtr1(ab[dt] + 4096 * e);
  if (i & 1) { A[dt][2] = v[0]; A[dt][3] = v[1]; } else { A[dt][0] = v[0]; A[dt][1] = v[1]; }
}
DI void readA(bf16x8 (&A)[4], const u32 (&ab)[4], int e) {
#pragma unroll
  for (int i = 0; i < 8; ++i) readA1(A, ab, e, i);
}
DI void readB(bf16x8 (&B)[5], u32 bb) {
#pragma unroll
  for (int qt = 0; qt < 5; ++qt) B[qt] = ld128(bb + qt * 2048);
}
constexpr int vpos[4] = {3, 8, 13, 18};
template <bool RF, bool LAST, int W1, int W0, bool NT = false, class Mid, class Ep>
DI void gstep(f32x4 (&acc)[4][5], bf16x8 (&A0)[4], bf16x8 (&B0)[5], bf16x8 (&B1)[5], u32 (&ab)[4], u32& slot, u32 b1,
              rsrc_t rr, u32 rsoff, const u32 (&rv)[8], Mid mid, Ep ep) {
  bf16x8 A1[4];
  sfor<20>([&](auto nc) AI {
    constexpr int n = nc.value, j = n == vpos[0] ? 0 : n == vpos[1] ? 1 : n == vpos[2] ? 2 : n == vpos[3] ? 3 : -1;
    if constexpr (RF && j >= 0) { if (j == 0) pin4(A0); set_m0<1024 * (j < 0 ? 0 : j)>(slot); SBAR(); }
    acc[n / 5][n % 5] = mfma(A0[n / 5], B0[n % 5], acc[n / 5][n % 5]);
    SBAR();
    if constexpr (RF && j >= 0 && VDMA) dma_m0<0, NT>(rr, rv[j < 0 ? 0 : j], rsoff);
    if constexpr (RF && VDMA == 2 && n % 5 == 0 && n > 0) dma_m0<0>(rr, rv[n / 5 - 1], rsoff);
    if (n < 5) B1[n % 5] = ld128(b1 + n * 2048);
    if (n == 5) wait_vm<W1>();
    if (n >= 5 && n < 13) readA1(A1, ab, 1, n - 5);
    if (n == 16) { ab[0] ^= 8192u; ab[1] ^= 8192u; asm volatile("" : "+v"(ab[0]), "+v"(ab[1])); }
    if (n == 17) { ab[2] ^= 8192u; ab[3] ^= 8192u; asm volatile("" : "+v"(ab[2]), "+v"(ab[3])); }
    SBAR();
  });
  const u32 b0 = mid();
  SBAR();
  sfor<20>([&](auto nc) AI {
    constexpr int n = nc.value, j = n == vpos[0] ? 4 : n == vpos[1] ? 5 : n == vpos[2] ? 6 : n == vpos[3] ? 7 : -1;
    constexpr int dt = LAST ? n % 4 : n / 5, qt = LAST ? n / 4 : n % 5;
    if constexpr (RF && j >= 0) { if (j == 4) pin4(A1); set_m0<1024 * (j < 0 ? 0 : j)>(slot); SBAR(); }
    acc[dt][qt] = mfma(A1[dt], B1[qt], acc[dt][qt]);
    SBAR();
    if constexpr (RF && j >= 0 && VDMA) dma_m0<0, NT>(rr, rv[j < 0 ? 0 : j], rsoff);
    if constexpr (RF && VDMA == 2 && n % 5 == 0) dma_m0<0>(rr, rv[n / 5 + 3], rsoff);
    if constexpr (RF && VDMA == 2 && n == 19) dma_m0<0>(rr, rv[7], rsoff);
    if constexpr (!LAST) {
      if (n < 5) B0[n % 5] = ld128(b0 + n * 2048);
      if (n == 5) wait_vm<W0>();
      if (n >= 5 && n < 13) readA1(A0, ab, 0, n - 5);
    } else if constexpr (n >= 2) ep(std::integral_constant<int, (n - 2) % 4>{}, std::integral_constant<int, (n - 2) / 4>{});
    SBAR();
  });
  if constexpr (LAST) { ep(std::integral_constant<int, 2>{}, std::integral_constant<int, 4>{}); ep(std::integral_constant<int, 3>{}, std::integral_constant<int, 4>{}); }
  slot ^= 8192u;
}
struct NoEp { template <class A, class B> DI void operator()(A, B) const {} };

constexpr int kpos[8] = {6, 11, 16, 21, 26, 31, 36, 38};
constexpr int kcount(int n) { int c = 0; for (int j = 0; j < 8; ++j) c += kpos[j] < n; return c; }
constexpr u32 kv_tile(u32 cu, u32 s) { if (KV_ORDER == 0) return s & 15; const u32 d = (cu - s) & 3; const u32 g = d == 0 ? 0 : (d == 1 ? 2 : 3); return (s - g) & 15; }
constexpr uint64_t kv_seq(u32 cu) { uint64_t t = 0; for (u32 i = 0; i < 16; ++i) t |= (uint64_t)kv_tile(cu, i) << (4 * i); return t; }

__global__ __launch_bounds__(512, 2) void fused_attn(const u16* __restrict__ x, const u16* __restrict__ kc, const u16* __restrict__ vc,
                                                     const u16* __restrict__ wq, const u16* __restrict__ wo, char* __restrict__ ws, u16* __restrict__ out) {
  const u32 lane = threadIdx.x & 63, wid = __builtin_amdgcn_readfirstlane(threadIdx.x >> 6), w = wid & 3;
  const u32 blk = blockIdx.x, kvh = blk & 7, idx = blk >> 3, b = idx >> 2, cu = idx & 3, head = 2 * kvh + (cu >> 1), half = cu & 1;
  ST(0); STR(40);
  const u32 q = lane & 15, g = lane >> 4;
  const u32 lr = lane >> 3, lc = (lane & 7) ^ (lr & 6);
  const u32 rot = 2 * b + half, rot7 = rot % 7;
  const uint64_t tseq = cu == 0 ? kv_seq(0) : cu == 1 ? kv_seq(1) : cu == 2 ? kv_seq(2) : kv_seq(3);
  auto tile_off = [&](u32 s) AI { return ((u32)(tseq >> (4 * s)) & 15) * (64 * 512); };
  const u32 grp = 2 * b + half;
  const u32 nb = wid < 4 ? w : 3 + w;
  auto wo_soff = [&](u32 j) AI { return ((rot + j) & 3) * 64 * (DM * 2) + nb * 128; };

  f32x4 acc[4][5];
  bf16x8 A0[4], B0[5], B1[5];
  u32 ab[4], rv[8], slot = wid < 4 ? L_KC + 16384 * w : L_VF + 16384 * w;
  auto init_ab = [&]() AI {
    const u32 lane = fresh_lane(), g = lane >> 4;
    const u32 q4 = (lane & 15) >> 2, p = lane & 3, f0 = (q4 & 2) ^ ((g & 1) << 2);
#pragma unroll
    for (int i = 0; i < 4; ++i) ab[i] = slot + g * 1024 + q4 * 128 + 16 * (((2 * i) ^ f0) | (p >> 1)) + 8 * (p & 1);
  };
  auto init_rv = [&](u32 stride) AI {
    const u32 lane = fresh_lane(), lr = lane >> 3, lc = (lane & 7) ^ (lr & 6);
#pragma unroll
    for (int i = 0; i < 8; ++i) rv[i] = (lr + 8 * i) * stride + ((lc * 16) ^ ((i & 1) * 64));
  };
  auto issue = [&](rsrc_t rr, u32 so, u32 dst) AI {
#pragma unroll
    for (int i = 0; i < 8; ++i) dma(rr, rv[i], so, dst + i * 1024);
  };
  auto zero_acc = [&]() AI {
#pragma unroll
    for (int dt = 0; dt < 4; ++dt)
#pragma unroll
      for (int qt = 0; qt < 5; ++qt) acc[dt][qt] = f32x4{0.f, 0.f, 0.f, 0.f};
  };
  const rsrc_t rWo = mkrsrc(wo + (size_t)head * DH * DM, DH * DM * 2);

  if (wid < 4) {
    {
      const rsrc_t rX = mkrsrc(x + (size_t)(b * SQ + half * ROWS) * DM, ROWS * DM * 2);
      const u32 voffX = lr * (DM * 2) + lc * 16;
      u32 v = rot7, dst = L_X;
#pragma unroll 1
      for (u32 vp = 0; vp < 7; ++vp) {
        dma(rX, voffX, w * 8 * (DM * 2) + v * 128, dst + w * 1024);
        dma(rX, voffX, (w + 4) * 8 * (DM * 2) + v * 128, dst + (w + 4) * 1024);
        if (w < 2) { dma(rX, voffX, (w + 8) * 8 * (DM * 2) + v * 128, dst + (w + 8) * 1024); wait_vm<3>(); } else wait_vm<2>();
        if (vp > 0) bar();
        v = v == 6 ? 0 : v + 1; dst += 10240;
      }
    }
    const rsrc_t rK = mkrsrc(kc + (size_t)(b * HKV + kvh) * SK * DH, KV_BYTES);
    const u32 vK0 = lr * 512 + lc * 16, vK1 = vK0 + 4096;
    ST(1);
    wait_vm<0>();
    bar();
    ST(2);
#pragma unroll 1
    for (u32 j = 0; j < 2; ++j) {
      const u32 so = tile_off(j) + 16 * w * 512, ks = slot + j * 8192;
#pragma unroll
      for (int v = 0; v < 4; ++v) { dma<(KV_NT & 1) != 0>(rK, vK0, so + 128 * v, ks + v * 1024); dma<(KV_NT & 1) != 0>(rK, vK1, so + 128 * v, ks + 4096 + v * 1024); }
    }
    u32 kb0 = slot + (q >> 3) * 4096 + (q & 7) * 128 + 16 * (g ^ (q & 6)), kb1 = kb0 ^ 64;
    u32 pw = L_P + (((2 * w + (g >> 1)) & 3) * 16 + q) * 16 + (g & 1) * 8 + (w >> 1) * 1024;
    asm volatile("" : "+v"(kb0), "+v"(kb1), "+v"(pw));
    float mref[5], l[5];
#pragma unroll
    for (int qt = 0; qt < 5; ++qt) { mref[qt] = 0.f; l[qt] = 0.f; }
    f32x4 S[2][5];
    bf16x8 kf[4];
    auto rdK = [&](int sl, int u) AI { return ld128(((u & 1) ? kb1 : kb0) + sl * 8192 + (u >> 1) * 1024); };
    ST(5);
    bar();
    ST(6);
    bf16x8 Q[8][5];
    auto rdQ = [&](int u, int qt) AI { Q[u][qt] = ld128(lane * 16 + (qt < 4 ? L_VF + 16384 * (u >> 1) + qt * 2048 : L_P + 2048 * (u >> 1)) + (u & 1) * 1024); };
    wait_vm<8>();
    sfor<8>([&](auto uc) AI {
      constexpr int u = uc.value;
      if (u == 0) { kf[0] = rdK(0, 0); rdQ(0, 0); rdQ(0, 1); rdQ(0, 2); rdQ(0, 3); rdQ(0, 4); }
      if (u < 7) { kf[(u + 1) & 1] = rdK(0, u + 1); rdQ((u + 1) & 7, 0); rdQ((u + 1) & 7, 1); rdQ((u + 1) & 7, 2); rdQ((u + 1) & 7, 3); rdQ((u + 1) & 7, 4); }
#pragma unroll
      for (int qt = 0; qt < 5; ++qt) S[0][qt] = mfma(kf[u & 1], Q[u][qt], u == 0 ? f32x4{0.f, 0.f, 0.f, 0.f} : S[0][qt]);
      SBAR();
    });
#pragma unroll
    for (int qt = 0; qt < 5; ++qt) S[1][qt] = f32x4{0.f, 0.f, 0.f, 0.f};
    u32 fla = L_FLAG + (lane & 3) * 4;
    u32 flw = L_FLAG + w * 4;
    asm volatile("" : "+v"(flw), "+v"(fla));
    *(LAS u32*)(flw + 16) = 0;
    bar_lds();
    {
      const u32 so = tile_off(2) + 16 * w * 512;
#pragma unroll
      for (int v = 0; v < 4; ++v) { dma<(KV_NT & 1) != 0>(rK, vK0, so + 128 * v, slot + v * 1024); dma<(KV_NT & 1) != 0>(rK, vK1, so + 128 * v, slot + 4096 + v * 1024); }
    }
    wait_vm<12>();
    kf[0] = rdK(1, 0); kf[1] = rdK(1, 1); kf[2] = rdK(1, 2);
    ST(7);

    auto adopt = [&](u32 par, bool s0) AI {
      const u32 q = fresh_lane() & 15;
      const u32x4 f4 = ldu4((flw & ~15u) + par * 16);
#pragma unroll
      for (int qt = 0; qt < 5; ++qt) {
        const f32x4 m4 = ldf4(L_MX + par * 1280 + (qt * 16 + q) * 16);
        const float own = mref[qt], base = s0 ? 0.f : own;
        float M = own;
#pragma unroll
        for (int i = 0; i < 4; ++i) M = fmaxf(M, f4[i] ? m4[i] : base);
        l[qt] *= ex2(own - M); mref[qt] = M;
        asm volatile("" : "+v"(l[qt]), "+v"(mref[qt]));
        SBAR();
      }
    };
    float t[4], ps[5];
    u32x2 pkd;
    float thr = -1.f;
    const u32 zero = 0;
    auto step = [&](auto xc, auto mc, u32 s) AI {
      constexpr int X = xc.value; constexpr bool MM = mc.value;
      u32 fl, so; float chk; uint64_t mask;
      sfor<40>([&](auto nc) AI {
        constexpr int n = nc.value, u = n / 5, qt = n % 5;
        constexpr bool kd = MM && kcount(n + 1) != kcount(n) && !NO_KDMA;
        constexpr int j = kcount(n), jv = j >> 1, jh = j & 1;
        if constexpr (kd) set_m0<(X ^ 1) * 8192 + jh * 4096 + jv * (1024 - 128)>(slot);
        if constexpr (MM) { if (u == 0) mfma_zero(S[X ^ 1][qt], kf[u & 3], Q[u][qt]); else mfma_acc(S[X ^ 1][qt], kf[u & 3], Q[u][qt]); }
        if constexpr (kd) dma_m0<128 * jv, (KV_NT & 1) != 0>(rK, jh ? vK1 : vK0, so);
        SBAR();
        if constexpr (MM && qt == 0) {
          if constexpr (u == 1 || u == 3) wait_vm<8 + 2 * (3 - (u + 3) / 2) + kcount(n + 1)>();
          if constexpr (u == 5 || u == 7) wait_vm<2 * (3 - (u - 5) / 2) + kcount(n + 1)>();
          kf[(u + 3) & 3] = rdK(u < 5 ? X ^ 1 : X, (u + 3) & 7);
        }
        if (n == 1) fl = *(const LAS u32*)(fla + (X ^ 1) * 16);
        if (MM && n == 2) {
          so = (s < 13 ? tile_off((s + 3) & 15) : (u32)(SK * DH * 2)) + 16 * w * 512;
          asm volatile("" : "+s"(so));
        }
        if constexpr (n >= 1 && n < 31) {
          constexpr int c = (n - 1) / 6, k = (n - 1) % 6;
          if (k == 0) { t[0] = fmaf(S[X][c][0], SCL, -mref[c]); t[1] = fmaf(S[X][c][1], SCL, -mref[c]); asm volatile("" : "+v"(t[0])); asm volatile("" : "+v"(t[1])); }
          if (k == 1) { t[2] = fmaf(S[X][c][2], SCL, -mref[c]); t[0] = ex2(t[0]); t[3] = fmaf(S[X][c][3], SCL, -mref[c]); asm volatile("" : "+v"(t[2])); asm volatile("" : "+v"(t[3])); }
          if (k == 2) { t[1] = ex2(t[1]); t[2] = ex2(t[2]); }
          if (k == 3) { t[3] = ex2(t[3]); pkd[0] = pk(t[0], t[1]); t[0] = fadd(t[0], t[1]); }
          if (k == 4) { pkd[1] = pk(t[2], t[3]); t[2] = fadd(t[2], t[3]); }
          if (k == 5) { if (NO_WR) asm volatile("" :: "v"(pkd)); else *(LAS u32x2*)(pw + X * 16384 + c * 2048) = pkd; ps[c] = fadd(t[0], t[2]); }
        }
        if (n == 26) { asm volatile("" : "+v"(fl)); chk = max3(ps[0], ps[1], __uint_as_float(fl)); }
        if (n == 28) { chk = max3(chk, ps[2], ps[3]); if (!NO_WR || s < 2) *(LAS u32*)(flw + X * 16) = zero; }
        if (n == 31) chk = max2(chk, ps[4]);
        if (n == 32) { mask = __builtin_amdgcn_ballot_w64(chk > thr); asm volatile("" : "+s"(mask)); }
        if (n == 33) {
          if (__builtin_expect(mask != 0, 0)) {
            const bool ad = __any(fl != 0), s0 = s == 0;
            if (ad) adopt(X ^ 1, s == 1);
            float mm = -__builtin_inff(), mn = __builtin_inff();
#pragma unroll
            for (int c = 0; c < 5; ++c) {
              const float m = fmaf(fmaxf(fmaxf(S[X][c][0], S[X][c][1]), fmaxf(S[X][c][2], S[X][c][3])), SCL, -mref[c]);
              mm = fmaxf(mm, m); mn = fminf(mn, m);
            }
            const bool move = __any(mm > LTHR || (s0 && mn < -LTHR));
            if (move || ad) {
              const u32 q = fresh_lane() & 15;
#pragma unroll
              for (int c = 0; c < 5; ++c) {
                float d = __builtin_ceilf(xmax(fmaf(fmaxf(fmaxf(S[X][c][0], S[X][c][1]), fmaxf(S[X][c][2], S[X][c][3])), SCL, -mref[c])));
                d = move ? (s0 ? d : fmaxf(d, 0.f)) : 0.f;
                l[c] *= ex2(fminf(-d, 0.f)); mref[c] += d;
                const float p0 = ex2(fmaf(S[X][c][0], SCL, -mref[c])), p1 = ex2(fmaf(S[X][c][1], SCL, -mref[c]));
                const float p2 = ex2(fmaf(S[X][c][2], SCL, -mref[c])), p3 = ex2(fmaf(S[X][c][3], SCL, -mref[c]));
                *(LAS u32x2*)(pw + X * 16384 + c * 2048) = u32x2{pk(p0, p1), pk(p2, p3)};
                ps[c] = (p0 + p1) + (p2 + p3);
                *(LAS float*)(L_MX + X * 1280 + (c * 16 + q) * 16 + w * 4) = mref[c];
                asm volatile("" : "+v"(ps[c]), "+v"(l[c]), "+v"(mref[c]));
                SBAR();
              }
              if (move) { u32 inf = 0x7f800000u; asm volatile("" : "+v"(inf)); *(LAS u32*)(flw + X * 16) = inf; }
            }
            thr = PTHR;
          }
        }
        if (n == 34) { l[0] = fadd(l[0], ps[0]); l[1] = fadd(l[1], ps[1]); }
        if (n == 36) { l[2] = fadd(l[2], ps[2]); l[3] = fadd(l[3], ps[3]); }
        if (n == 37) l[4] = fadd(l[4], ps[4]);
        SBAR();
      });
      if constexpr (MM) asm volatile("s_waitcnt lgkmcnt(1)\n\ts_barrier" ::: "memory");
      else bar_lds();
    };
    using I0 = std::integral_constant<int, 0>; using I1 = std::integral_constant<int, 1>;
    step(I0{}, I1{}, 0);
    ST(9); STR(41);
#pragma unroll 1
    for (u32 s = 1; s < 15; s += 2) {
      step(I1{}, I1{}, s); step(I0{}, I1{}, s + 1);
    }
    ST(10); STR(42);
    wait_vm<0>();
    asm volatile("s_nop 7" ::: "memory");
    step(I1{}, I0{}, 15);
    if (__any(*(const LAS u32*)(fla + 16) != 0)) adopt(1, false);
    { const u32 q = fresh_lane() & 15;
#pragma unroll
      for (int qt = 0; qt < 5; ++qt) *(LAS float*)(L_LS + (qt * 16 + q) * 16 + w * 4) = xsum(l[qt]);
    }
    init_rv(DM * 2);
    if (w < 3) { issue(rWo, wo_soff(0), slot); issue(rWo, wo_soff(1), slot + 8192); }
    bar_lds();
    init_ab();
    wait_vm<12>();
    readA(A0, ab, 0);
    zero_acc();
    ST(58);
    bar();
    ST(59);
  } else {
    const rsrc_t rWq = mkrsrc(wq + head * DH, (DM * HQ * DH - head * DH) * 2);
    auto wq_soff = [&](u32 vp) AI { u32 v = vp + rot7; v = v >= 7 ? v - 7 : v; return v * 64 * (HQ * DH * 2) + w * 128; };
    init_rv(HQ * DH * 2);
    issue(rWq, wq_soff(0), slot);
    init_ab();
    zero_acc();
    SBAR();
    issue(rWq, wq_soff(1), slot + 8192);
    const rsrc_t rV = mkrsrc(vc + (size_t)(b * HKV + kvh) * SK * DH, KV_BYTES);
    const u32 wa = ((g >> 1) * 16 + q) * 16 + (g & 1) * 8;
    wait_vm<12>();
    readA(A0, ab, 0);
    {
      u32 xb = L_X + (q >> 3) * 1024 + (q & 7) * 128 + 16 * (g ^ (q & 6));
      bar();
      ST(1);
      readB(B0, xb);
      auto mid = [&]() AI { pin5(B1); bar();   xb += 10240; return xb; };
#pragma unroll 1
      for (u32 vp = 0; vp < 5; ++vp) gstep<true, false, 9, 9>(acc, A0, B0, B1, ab, slot, xb ^ 64, rWq, wq_soff(vp + 2), rv, mid, NoEp{});
      ST(2);
      gstep<false, false, 8, 4>(acc, A0, B0, B1, ab, slot, xb ^ 64, rWq, 0, rv, mid, NoEp{});
      ST(3);
      const u32 qa = slot + wa, qa2 = L_P + 2048 * w + wa;
      gstep<false, true, 0, 0>(acc, A0, B0, B1, ab, slot, xb ^ 64, rWq, 0, rv, [&]() AI { return 0u; }, [&](auto dtc, auto qtc) AI {
        constexpr int dt = dtc.value, qt = qtc.value;
        const f32x4 v = acc[dt][qt];
        *(LAS u32x2*)((qt < 4 ? qa + qt * 2048 : qa2) + (dt >> 1) * 1024 + (dt & 1) * 512) = u32x2{pk(v[0], v[1]), pk(v[2], v[3])};
      });
      ST(4);
      bar_lds();
      init_rv(512);
      issue(rV, tile_off(0) + w * 128, slot);
      zero_acc();
      bar();
      issue(rV, tile_off(1) + w * 128, slot ^ 8192u);
      wait_vm<12>();
      readA(A0, ab, 0);
      ST(5);
    }
    float mref[5];
#pragma unroll
    for (int qt = 0; qt < 5; ++qt) mref[qt] = 0.f;
    u32 pb = L_P + lane * 16;
    u32 fla = L_FLAG + (lane & 3) * 4;
    bar();
    ST(6);
    u32 fl = *(const LAS u32*)fla;
    readB(B0, pb);
    rsrc_t rr = rV;
    float f1[5];
    bool rare;
    auto pre = [&](u32 s) AI {
      rare = __any(fl != 0);
      if (__builtin_expect(rare, 0)) {
        const u32x4 f4 = ldu4(L_FLAG + (s & 1) * 16);
#pragma unroll
        for (int qt = 0; qt < 5; ++qt) {
          const f32x4 m4 = ldf4(L_MX + (s & 1) * 1280 + (qt * 16 + q) * 16);
          float r[4];
#pragma unroll
          for (int i = 0; i < 4; ++i) r[i] = f4[i] ? m4[i] : mref[qt];
          const float M = fmaxf(fmaxf(r[0], r[1]), fmaxf(r[2], r[3]));
          const float f = ex2(fminf(mref[qt] - M, 0.f));
#pragma unroll
          for (int dt = 0; dt < 4; ++dt) acc[dt][qt] *= f;
          B0[qt] = scale8(B0[qt], ex2(((g >> 1) ? r[1] : r[0]) - M));
          f1[qt] = ex2(((g >> 1) ? r[3] : r[2]) - M);
          mref[qt] = M;
        }
      }
    };
    auto mid = [&]() AI {
      pb ^= 16384u; fla ^= 16u;
      pin5(B1);
      if (__builtin_expect(rare, 0)) {
#pragma unroll
        for (int qt = 0; qt < 5; ++qt) B1[qt] = scale8(B1[qt], f1[qt]);
      }
      bar();
    };
#pragma unroll 1
    for (u32 s = 0; s < 15; ++s) {
      if (NO_PV) { bar(); continue; }
      u32 rsoff = tile_off((s + 2) & 15) + w * 128;
      if (__builtin_expect(s == 14, 0)) { rr = rWo; rsoff = wo_soff(0); init_rv(DM * 2); }
      pre(s);
      gstep<true, false, 9, 9, (KV_NT & 2) != 0>(acc, A0, B0, B1, ab, slot, pb + 1024, rr, rsoff, rv, [&]() AI { mid(); fl = *(const LAS u32*)fla; return pb; }, NoEp{});
    }
    ST(56);
    pre(15);
    float inv[5];
    const u32 oa = L_O + w * 10240 + wa;
    gstep<true, true, 9, 0>(acc, A0, B0, B1, ab, slot, pb + 1024, rWo, wo_soff(1), rv, [&]() AI {
      mid();
#pragma unroll
      for (int qt = 0; qt < 5; ++qt) { const f32x4 l4 = ldf4(L_LS + (qt * 16 + q) * 16); inv[qt] = __builtin_amdgcn_rcpf((l4[0] + l4[1]) + (l4[2] + l4[3])); }
      return 0u;
    }, [&](auto dtc, auto qtc) AI {
      constexpr int dt = dtc.value, qt = qtc.value;
      const f32x4 v = acc[dt][qt] * inv[qt];
      *(LAS u32x2*)(oa + qt * 2048 + (dt >> 1) * 1024 + (dt & 1) * 512) = u32x2{pk(v[0], v[1]), pk(v[2], v[3])};
    });
    wait_vm<12>();
    readA(A0, ab, 0);
    zero_acc();
    ST(58);
    asm volatile("s_waitcnt lgkmcnt(8)\n\ts_barrier" ::: "memory");
    ST(59);
  }
  if (wid != 3) {
    const u32 lane = fresh_lane();
    slot = wid < 4 ? L_KC + 16384 * w : L_VF + 16384 * w + 8192;
    auto ob = [&](u32 j) AI { return L_O + ((rot + j) & 3) * 10240 + lane * 16; };
    readB(B0, ob(0));
#pragma unroll 1
    for (u32 j = 0; j < 2; ++j)
      gstep<true, false, 9, 9>(acc, A0, B0, B1, ab, slot, ob(j) + 1024, rWo, wo_soff(j + 2), rv, [&]() AI { return ob(j + 1); }, NoEp{});
    gstep<false, false, 8, 4>(acc, A0, B0, B1, ab, slot, ob(2) + 1024, rWo, 0, rv, [&]() AI { return ob(3); }, NoEp{});
    ST(60);
    const __amdgpu_buffer_rsrc_t rP = __builtin_amdgcn_make_buffer_rsrc(ws + W_PART + (size_t)(grp * HQ + head) * PART_BYTES, (short)0, PART_BYTES, 0x00020000);
    const u32 po = nb * 10240 + lane * 16;
    gstep<false, true, 0, 0>(acc, A0, B0, B1, ab, slot, ob(3) + 1024, rWo, 0, rv, [&]() AI { return 0u; }, [&](auto dtc, auto qtc) AI {
      constexpr int dt = dtc.value, qt = qtc.value;
      if constexpr (dt & 1) {
        const f32x4 a = acc[dt - 1][qt], c = acc[dt][qt];
        __builtin_amdgcn_raw_buffer_store_b128(u32x4{pk(a[0], a[1]), pk(a[2], a[3]), pk(c[0], c[1]), pk(c[2], c[3])}, rP, po, (qt * 2 + (dt >> 1)) * 1024, 16);
      }
    });
    ST(61);
  }
  unsigned long long tag = 0;
  if (wid == 3) tag = __hip_atomic_load((gu64*)(ws + W_EPOCH + grp * 128), __ATOMIC_RELAXED, __HIP_MEMORY_SCOPE_AGENT) + 1;
  wait_vm<0>();
  ST(62);
  bar();
  {
    const u32 lane = fresh_lane();
    gu64* flags = (gu64*)(ws + W_FLAG + grp * HQ * 128);
    if (wid == 3) {
      if (lane == 0) __hip_atomic_store(flags + head * 16, tag, __ATOMIC_RELAXED, __HIP_MEMORY_SCOPE_AGENT);
#pragma unroll 1
      for (u32 spins = 0; spins < (1u << 24); ++spins) {
        const unsigned long long f = __hip_atomic_load(flags + (lane & 15) * 16, __ATOMIC_RELAXED, __HIP_MEMORY_SCOPE_AGENT);
        if (__all(f == tag)) break;
        __builtin_amdgcn_s_sleep(1);
      }
      if (head == 0 && lane == 0) __hip_atomic_store((gu64*)(ws + W_EPOCH + grp * 128), tag, __ATOMIC_RELAXED, __HIP_MEMORY_SCOPE_AGENT);
    }
    bar();
    ST(63);
    const u32 tid = wid * 64 + lane;
    if (tid < 280) {
      const __amdgpu_buffer_rsrc_t rG = __builtin_amdgcn_make_buffer_rsrc(ws + W_PART + (size_t)grp * HQ * PART_BYTES, (short)0, HQ * PART_BYTES, 0x00020000);
      const u32 t = head * 280 + tid, ln = t & 63, unit = t >> 6;
      u32x4 v[16];
#pragma unroll
      for (int h = 0; h < 16; ++h) v[h] = __builtin_amdgcn_raw_buffer_load_b128(rG, t * 16, h * PART_BYTES, 16);
      float a[8] = {0.f, 0.f, 0.f, 0.f, 0.f, 0.f, 0.f, 0.f};
#pragma unroll
      for (int h = 0; h < 16; ++h)
#pragma unroll
        for (int i = 0; i < 4; ++i) { a[2 * i] += __uint_as_float(v[h][i] << 16); a[2 * i + 1] += __uint_as_float(v[h][i] & 0xffff0000u); }
      const u32 ub = unit / 10, r = unit - 10 * ub;
      u16* o = out + (size_t)(grp * ROWS + (r >> 1) * 16 + (ln & 15)) * DM + ub * 64 + (r & 1) * 32 + 4 * (ln >> 4);
      *(u32x2*)o = u32x2{pk(a[0], a[1]), pk(a[2], a[3])};
      *(u32x2*)(o + 16) = u32x2{pk(a[4], a[5]), pk(a[6], a[7])};
    }
  }
  ST_OUT();
}

extern "C" void kernel_launch(void* const* d_in, const int* in_sizes, int n_in, void* d_out, int out_size, void* d_ws, size_t ws_size,
                              hipStream_t stream) {
  (void)in_sizes; (void)n_in; (void)out_size; (void)ws_size;
  (void)hipFuncSetAttribute((const void*)fused_attn, hipFuncAttributeMaxDynamicSharedMemorySize, (int)L_END);
  hipLaunchKernelGGL(fused_attn, dim3(256), dim3(512), L_END, stream, (const u16*)d_in[0], (const u16*)d_in[1], (const u16*)d_in[2],
                     (const u16*)d_in[3], (const u16*)d_in[4], (char*)d_ws, (u16*)d_out);
}
```

```cpp
#include <hip/hip_runtime.h>
#include <stdint.h>
#include <utility>

typedef unsigned int u32;
typedef unsigned short u16;
typedef __bf16 bf16x8_t __attribute__((ext_vector_type(8)));
typedef __bf16 bf16x2 __attribute__((ext_vector_type(2)));
typedef short s16x4 __attribute__((ext_vector_type(4)));
typedef float f32x4 __attribute__((ext_vector_type(4)));
typedef float f32x2 __attribute__((ext_vector_type(2)));
typedef u32 u32x2 __attribute__((ext_vector_type(2)));
typedef u32 u32x4 __attribute__((ext_vector_type(4)));
typedef u32x4 rsrc_t;
typedef u32x4 bf16x8;
#define LAS __attribute__((address_space(3)))
#define DI __device__ __forceinline__
#define AI __attribute__((always_inline))
#define SBAR() __builtin_amdgcn_sched_barrier(0)

template <class F, int... Is> DI void sfor_(F&& f, std::integer_sequence<int, Is...>) { (f(std::integral_constant<int, Is>{}), ...); }
template <int N, class F> DI void sfor(F&& f) { sfor_(f, std::make_integer_sequence<int, N>{}); }

constexpr int SQ = 160, DM = 448, HQ = 16, HKV = 8, SK = 1024, DH = 256;
constexpr int ROWS = 80;
constexpr float SCL = 0.0625f * 1.4426950408889634f;
constexpr float PTHR = 1048576.0f;
constexpr float LTHR = 18.0f;

constexpr u32 L_VF = 0;
constexpr u32 L_KC = 65536;
constexpr u32 L_X = 90112;
constexpr u32 L_P = 131072;
constexpr u32 L_MX = 141312;
constexpr u32 L_FLAG = 143872;
constexpr u32 L_LS = L_KC + 6 * 8192;
constexpr u32 L_O = 122880;
constexpr u32 L_END = 163840;
constexpr u32 W_EPOCH = 0, W_FLAG = 16 * 128, W_PART = 65536, PART_BYTES = 70 * 1024;
typedef __attribute__((address_space(1))) unsigned long long gu64;

#define KV_NT 0
#define KV_ORDER 1
template <bool NT = false> DI void dma(rsrc_t r, u32 voff, u32 soff, u32 ldsaddr) {
  if (NT) asm volatile("s_mov_b32 m0, %0\n\ts_nop 0\n\tbuffer_load_dwordx4 %1, %2, %3 offen nt lds" ::"s"(ldsaddr), "v"(voff), "s"(r), "s"(soff) : "memory");
  else asm volatile("s_mov_b32 m0, %0\n\ts_nop 0\n\tbuffer_load_dwordx4 %1, %2, %3 offen lds" ::"s"(ldsaddr), "v"(voff), "s"(r), "s"(soff) : "memory");
}
template <int LOFF> DI void set_m0(u32 lds) { asm volatile("s_add_u32 m0, %0, %1" ::"s"(lds), "i"(LOFF) : "memory", "scc"); }
template <int IMM, bool NT = false> DI void dma_m0(rsrc_t r, u32 voff, u32 soff) {
  if (NT) asm volatile("buffer_load_dwordx4 %0, %1, %2 offen offset:%3 nt lds" ::"v"(voff), "s"(r), "s"(soff), "i"(IMM) : "memory");
  else asm volatile("buffer_load_dwordx4 %0, %1, %2 offen offset:%3 lds" ::"v"(voff), "s"(r), "s"(soff), "i"(IMM) : "memory");
}
DI void mfma_acc(f32x4& c, const bf16x8& a, const bf16x8& b) { asm volatile("v_mfma_f32_16x16x32_bf16 %0, %1, %2, %0" : "+v"(c) : "v"(a), "v"(b)); }
DI void mfma_zero(f32x4& d, const bf16x8& a, const bf16x8& b) { asm volatile("v_mfma_f32_16x16x32_bf16 %0, %1, %2, 0" : "+v"(d) : "v"(a), "v"(b)); }
DI void pin4(const bf16x8 (&A)[4]) { asm volatile("" ::"v"(A[0]), "v"(A[1]), "v"(A[2]), "v"(A[3]) : "memory"); }
DI void pin5(const bf16x8 (&A)[5]) { asm volatile("" ::"v"(A[0]), "v"(A[1]), "v"(A[2]), "v"(A[3]), "v"(A[4]) : "memory"); }
template <int N> DI void wait_vm() { asm volatile("s_waitcnt vmcnt(%0)" ::"i"(N) : "memory"); }
DI void bar() { asm volatile("s_barrier" ::: "memory"); }
DI void bar_lds() { asm volatile("s_waitcnt lgkmcnt(0)\n\ts_barrier" ::: "memory"); }
DI bf16x8 ld128(u32 a) { return *(const LAS bf16x8*)a; }
DI f32x4 ldf4(u32 a) { return *(const LAS f32x4*)a; }
DI u32x4 ldu4(u32 a) { return *(const LAS u32x4*)a; }
DI u32x2 ldtr1(u32 a) { return __builtin_bit_cast(u32x2, __builtin_amdgcn_ds_read_tr16_b64_v4i16((LAS s16x4*)a)); }
DI u32 pk(float lo, float hi) { f32x2 v = {lo, hi}; return __builtin_bit_cast(u32, __builtin_convertvector(v, bf16x2)); }
DI f32x4 mfma(bf16x8 a, bf16x8 b, f32x4 c) { return __builtin_amdgcn_mfma_f32_16x16x32_bf16(__builtin_bit_cast(bf16x8_t, a), __builtin_bit_cast(bf16x8_t, b), c, 0, 0, 0); }
DI rsrc_t mkrsrc(const void* p, u32 bytes) { const uint64_t a = (uint64_t)p; return rsrc_t{(u32)a, (u32)(a >> 32) & 0xffffu, bytes, 0x00020000u}; }
DI u32 fresh_lane() { u32 l; asm volatile("v_mbcnt_lo_u32_b32 %0, -1, 0\n\tv_mbcnt_hi_u32_b32 %0, -1, %0" : "=v"(l)); return l; }
DI float ex2(float x) { return __builtin_amdgcn_exp2f(x); }
DI float max3(float a, float b, float c) { float r; asm("v_max3_f32 %0, %1, %2, %3" : "=v"(r) : "v"(a), "v"(b), "v"(c)); return r; }
DI float max2(float a, float b) { float r; asm("v_max_f32_e32 %0, %1, %2" : "=v"(r) : "v"(a), "v"(b)); return r; }
DI float fadd(float a, float b) { float r; asm("v_add_f32_e32 %0, %1, %2" : "=v"(r) : "v"(a), "v"(b)); return r; }
DI float xmax(float v) {
  auto a = __builtin_amdgcn_permlane16_swap(__float_as_uint(v), __float_as_uint(v), false, false);
  v = fmaxf(__uint_as_float(a[0]), __uint_as_float(a[1]));
  auto b = __builtin_amdgcn_permlane32_swap(__float_as_uint(v), __float_as_uint(v), false, false);
  return fmaxf(__uint_as_float(b[0]), __uint_as_float(b[1]));
}
DI float xsum(float v) {
  auto a = __builtin_amdgcn_permlane16_swap(__float_as_uint(v), __float_as_uint(v), false, false);
  v = __uint_as_float(a[0]) + __uint_as_float(a[1]);
  auto b = __builtin_amdgcn_permlane32_swap(__float_as_uint(v), __float_as_uint(v), false, false);
  return __uint_as_float(b[0]) + __uint_as_float(b[1]);
}
DI bf16x8 scale8(bf16x8 u, float f) {
#pragma unroll
  for (int i = 0; i < 4; ++i) {
    const float lo = __uint_as_float(u[i] << 16) * f, hi = __uint_as_float(u[i] & 0xffff0000u) * f;
    u32 r; asm("v_cvt_pk_bf16_f32 %0, %1, %2" : "=v"(r) : "v"(lo), "v"(hi));
    u[i] = r;
  }
  asm volatile("s_nop 1" : "+v"(u));
  return u;
}

#define ST(i)
#define STR(i)
#define ST_OUT()
#define NO_PV 0
#define PRETOUCH 2
#define NO_WR 0
#define KV_BYTES (SK * DH * 2)
#define NO_KDMA 0
#define VDMA 1

DI void readA1(bf16x8 (&A)[4], const u32 (&ab)[4], int e, int i) {
  const int dt = i >> 1;
  const u32x2 v = (i & 1) ? ldtr1(ab[dt ^ 2] + 4096 * e + 512) : ldtr1(ab[dt] + 4096 * e);
  if (i & 1) { A[dt][2] = v[0]; A[dt][3] = v[1]; } else { A[dt][0] = v[0]; A[dt][1] = v[1]; }
}
DI void readA(bf16x8 (&A)[4], const u32 (&ab)[4], int e) {
#pragma unroll
  for (int i = 0; i < 8; ++i) readA1(A, ab, e, i);
}
DI void readB(bf16x8 (&B)[5], u32 bb) {
#pragma unroll
  for (int qt = 0; qt < 5; ++qt) B[qt] = ld128(bb + qt * 2048);
}
constexpr int vpos[4] = {3, 8, 13, 18};
template <bool RF, bool LAST, int W1, int W0, bool NT = false, class Mid, class Ep>
DI void gstep(f32x4 (&acc)[4][5], bf16x8 (&A0)[4], bf16x8 (&B0)[5], bf16x8 (&B1)[5], u32 (&ab)[4], u32& slot, u32 b1,
              rsrc_t rr, u32 rsoff, const u32 (&rv)[8], Mid mid, Ep ep) {
  bf16x8 A1[4];
  sfor<20>([&](auto nc) AI {
    constexpr int n = nc.value, j = n == vpos[0] ? 0 : n == vpos[1] ? 1 : n == vpos[2] ? 2 : n == vpos[3] ? 3 : -1;
    if constexpr (RF && j >= 0) { if (j == 0) pin4(A0); set_m0<1024 * (j < 0 ? 0 : j)>(slot); SBAR(); }
    acc[n / 5][n % 5] = mfma(A0[n / 5], B0[n % 5], acc[n / 5][n % 5]);
    SBAR();
    if constexpr (RF && j >= 0 && VDMA) dma_m0<0, NT>(rr, rv[j < 0 ? 0 : j], rsoff);
    if constexpr (RF && VDMA == 2 && n % 5 == 0 && n > 0) dma_m0<0>(rr, rv[n / 5 - 1], rsoff);
    if (n < 5) B1[n % 5] = ld128(b1 + n * 2048);
    if (n == 5) wait_vm<W1>();
    if (n >= 5 && n < 13) readA1(A1, ab, 1, n - 5);
    if (n == 16) { ab[0] ^= 8192u; ab[1] ^= 8192u; asm volatile("" : "+v"(ab[0]), "+v"(ab[1])); }
    if (n == 17) { ab[2] ^= 8192u; ab[3] ^= 8192u; asm volatile("" : "+v"(ab[2]), "+v"(ab[3])); }
    SBAR();
  });
  const u32 b0 = mid();
  SBAR();
  sfor<20>([&](auto nc) AI {
    constexpr int n = nc.value, j = n == vpos[0] ? 4 : n == vpos[1] ? 5 : n == vpos[2] ? 6 : n == vpos[3] ? 7 : -1;
    constexpr int dt = LAST ? n % 4 : n / 5, qt = LAST ? n / 4 : n % 5;
    if constexpr (RF && j >= 0) { if (j == 4) pin4(A1); set_m0<1024 * (j < 0 ? 0 : j)>(slot); SBAR(); }
    acc[dt][qt] = mfma(A1[dt], B1[qt], acc[dt][qt]);
    SBAR();
    if constexpr (RF && j >= 0 && VDMA) dma_m0<0, NT>(rr, rv[j < 0 ? 0 : j], rsoff);
    if constexpr (RF && VDMA == 2 && n % 5 == 0) dma_m0<0>(rr, rv[n / 5 + 3], rsoff);
    if constexpr (RF && VDMA == 2 && n == 19) dma_m0<0>(rr, rv[7], rsoff);
    if constexpr (!LAST) {
      if (n < 5) B0[n % 5] = ld128(b0 + n * 2048);
      if (n == 5) wait_vm<W0>();
      if (n >= 5 && n < 13) readA1(A0, ab, 0, n - 5);
    } else if constexpr (n >= 2) ep(std::integral_constant<int, (n - 2) % 4>{}, std::integral_constant<int, (n - 2) / 4>{});
    SBAR();
  });
  if constexpr (LAST) { ep(std::integral_constant<int, 2>{}, std::integral_constant<int, 4>{}); ep(std::integral_constant<int, 3>{}, std::integral_constant<int, 4>{}); }
  slot ^= 8192u;
}
struct NoEp { template <class A, class B> DI void operator()(A, B) const {} };

constexpr int kpos[8] = {6, 11, 16, 21, 26, 31, 36, 38};
constexpr int kcount(int n) { int c = 0; for (int j = 0; j < 8; ++j) c += kpos[j] < n; return c; }
constexpr u32 kv_tile(u32 cu, u32 s) { if (KV_ORDER == 0) return s & 15; const u32 d = (cu - s) & 3; const u32 g = d == 0 ? 0 : (d == 1 ? 2 : 3); return (s - g) & 15; }
constexpr uint64_t kv_seq(u32 cu) { uint64_t t = 0; for (u32 i = 0; i < 16; ++i) t |= (uint64_t)kv_tile(cu, i) << (4 * i); return t; }

__global__ __launch_bounds__(512, 2) void fused_attn(const u16* __restrict__ x, const u16* __restrict__ kc, const u16* __restrict__ vc,
                                                     const u16* __restrict__ wq, const u16* __restrict__ wo, char* __restrict__ ws, u16* __restrict__ out) {
  uint64_t pc0; asm volatile("s_getpc_b64 %0" : "=s"(pc0));
  const u32 lane = threadIdx.x & 63, wid = __builtin_amdgcn_readfirstlane(threadIdx.x >> 6), w = wid & 3;
  const u32 blk = blockIdx.x, kvh = blk & 7, idx = blk >> 3, b = idx >> 2, cu = idx & 3, head = 2 * kvh + (cu >> 1), half = cu & 1;
  ST(0); STR(40);
  const u32 q = lane & 15, g = lane >> 4;
  const u32 lr = lane >> 3, lc = (lane & 7) ^ (lr & 6);
  const u32 rot = 2 * b + half, rot7 = rot % 7;
  const uint64_t tseq = cu == 0 ? kv_seq(0) : cu == 1 ? kv_seq(1) : cu == 2 ? kv_seq(2) : kv_seq(3);
  auto tile_off = [&](u32 s) AI { return ((u32)(tseq >> (4 * s)) & 15) * (64 * 512); };
  const u32 grp = 2 * b + half;
  const u32 nb = wid < 4 ? w : 3 + w;
  auto wo_soff = [&](u32 j) AI { return ((rot + j) & 3) * 64 * (DM * 2) + nb * 128; };

  f32x4 acc[4][5];
  bf16x8 A0[4], B0[5], B1[5];
  u32 ab[4], rv[8], slot = wid < 4 ? L_KC + 16384 * w : L_VF + 16384 * w;
  auto init_ab = [&]() AI {
    const u32 lane = fresh_lane(), g = lane >> 4;
    const u32 q4 = (lane & 15) >> 2, p = lane & 3, f0 = (q4 & 2) ^ ((g & 1) << 2);
#pragma unroll
    for (int i = 0; i < 4; ++i) ab[i] = slot + g * 1024 + q4 * 128 + 16 * (((2 * i) ^ f0) | (p >> 1)) + 8 * (p & 1);
  };
  auto init_rv = [&](u32 stride) AI {
    const u32 lane = fresh_lane(), lr = lane >> 3, lc = (lane & 7) ^ (lr & 6);
#pragma unroll
    for (int i = 0; i < 8; ++i) rv[i] = (lr + 8 * i) * stride + ((lc * 16) ^ ((i & 1) * 64));
  };
  auto issue = [&](rsrc_t rr, u32 so, u32 dst) AI {
#pragma unroll
    for (int i = 0; i < 8; ++i) dma(rr, rv[i], so, dst + i * 1024);
  };
  auto zero_acc = [&]() AI {
#pragma unroll
    for (int dt = 0; dt < 4; ++dt)
#pragma unroll
      for (int qt = 0; qt < 5; ++qt) acc[dt][qt] = f32x4{0.f, 0.f, 0.f, 0.f};
  };
  const rsrc_t rWo = mkrsrc(wo + (size_t)head * DH * DM, DH * DM * 2);

  if (wid < 4) {
    if (w == 2) {
      const char* pp = (const char*)(pc0 & ~(uint64_t)127) + (idx * 8 + (lane & 7)) * 128;
      asm volatile("s_mov_b32 m0, %1\n\ts_nop 0\n\tglobal_load_lds_dword %0, off" ::"v"(pp), "s"(L_KC + w * 256) : "memory");
    }
    {
      const rsrc_t rX = mkrsrc(x + (size_t)(b * SQ + half * ROWS) * DM, ROWS * DM * 2);
      const u32 voffX = lr * (DM * 2) + lc * 16;
      u32 v = rot7, dst = L_X;
#pragma unroll 1
      for (u32 vp = 0; vp < 7; ++vp) {
        dma(rX, voffX, w * 8 * (DM * 2) + v * 128, dst + w * 1024);
        dma(rX, voffX, (w + 4) * 8 * (DM * 2) + v * 128, dst + (w + 4) * 1024);
        if (w < 2) { dma(rX, voffX, (w + 8) * 8 * (DM * 2) + v * 128, dst + (w + 8) * 1024); wait_vm<3>(); } else wait_vm<2>();
        if (vp > 0) bar();
        v = v == 6 ? 0 : v + 1; dst += 10240;
      }
    }
    const rsrc_t rK = mkrsrc(kc + (size_t)(b * HKV + kvh) * SK * DH, KV_BYTES);
    const u32 vK0 = lr * 512 + lc * 16, vK1 = vK0 + 4096;
    ST(1);
    wait_vm<0>();
    bar();
    ST(2);
#pragma unroll 1
    for (u32 j = 0; j < 2; ++j) {
      const u32 so = tile_off(j) + 16 * w * 512, ks = slot + j * 8192;
#pragma unroll
      for (int v = 0; v < 4; ++v) { dma<(KV_NT & 1) != 0>(rK, vK0, so + 128 * v, ks + v * 1024); dma<(KV_NT & 1) != 0>(rK, vK1, so + 128 * v, ks + 4096 + v * 1024); }
    }
    u32 kb0 = slot + (q >> 3) * 4096 + (q & 7) * 128 + 16 * (g ^ (q & 6)), kb1 = kb0 ^ 64;
    u32 pw = L_P + (((2 * w + (g >> 1)) & 3) * 16 + q) * 16 + (g & 1) * 8 + (w >> 1) * 1024;
    asm volatile("" : "+v"(kb0), "+v"(kb1), "+v"(pw));
    float mref[5], l[5];
#pragma unroll
    for (int qt = 0; qt < 5; ++qt) { mref[qt] = 0.f; l[qt] = 0.f; }
    f32x4 S[2][5];
    bf16x8 kf[4];
    auto rdK = [&](int sl, int u) AI { return ld128(((u & 1) ? kb1 : kb0) + sl * 8192 + (u >> 1) * 1024); };
    ST(5);
    bar();
    ST(6);
    bf16x8 Q[8][5];
    auto rdQ = [&](int u, int qt) AI { Q[u][qt] = ld128(lane * 16 + (qt < 4 ? L_VF + 16384 * (u >> 1) + qt * 2048 : L_P + 2048 * (u >> 1)) + (u & 1) * 1024); };
    wait_vm<8>();
    sfor<8>([&](auto uc) AI {
      constexpr int u = uc.value;
      if (u == 0) { kf[0] = rdK(0, 0); rdQ(0, 0); rdQ(0, 1); rdQ(0, 2); rdQ(0, 3); rdQ(0, 4); }
      if (u < 7) { kf[(u + 1) & 1] = rdK(0, u + 1); rdQ((u + 1) & 7, 0); rdQ((u + 1) & 7, 1); rdQ((u + 1) & 7, 2); rdQ((u + 1) & 7, 3); rdQ((u + 1) & 7, 4); }
#pragma unroll
      for (int qt = 0; qt < 5; ++qt) S[0][qt] = mfma(kf[u & 1], Q[u][qt], u == 0 ? f32x4{0.f, 0.f, 0.f, 0.f} : S[0][qt]);
      SBAR();
    });
#pragma unroll
    for (int qt = 0; qt < 5; ++qt) S[1][qt] = f32x4{0.f, 0.f, 0.f, 0.f};
    u32 fla = L_FLAG + (lane & 3) * 4;
    u32 flw = L_FLAG + w * 4;
    asm volatile("" : "+v"(flw), "+v"(fla));
    *(LAS u32*)(flw + 16) = 0;
    bar_lds();
    {
      const u32 so = tile_off(2) + 16 * w * 512;
#pragma unroll
      for (int v = 0; v < 4; ++v) { dma<(KV_NT & 1) != 0>(rK, vK0, so + 128 * v, slot + v * 1024); dma<(KV_NT & 1) != 0>(rK, vK1, so + 128 * v, slot + 4096 + v * 1024); }
    }
    wait_vm<12>();
    kf[0] = rdK(1, 0); kf[1] = rdK(1, 1); kf[2] = rdK(1, 2);
    ST(7);

    auto adopt = [&](u32 par, bool s0) AI {
      const u32 q = fresh_lane() & 15;
      const u32x4 f4 = ldu4((flw & ~15u) + par * 16);
#pragma unroll
      for (int qt = 0; qt < 5; ++qt) {
        const f32x4 m4 = ldf4(L_MX + par * 1280 + (qt * 16 + q) * 16);
        const float own = mref[qt], base = s0 ? 0.f : own;
        float M = own;
#pragma unroll
        for (int i = 0; i < 4; ++i) M = fmaxf(M, f4[i] ? m4[i] : base);
        l[qt] *= ex2(own - M); mref[qt] = M;
        asm volatile("" : "+v"(l[qt]), "+v"(mref[qt]));
        SBAR();
      }
    };
    float t[4], ps[5];
    u32x2 pkd;
    float thr = -1.f;
    const u32 zero = 0;
    auto step = [&](auto xc, auto mc, u32 s) AI {
      constexpr int X = xc.value; constexpr bool MM = mc.value;
      u32 fl, so; float chk; uint64_t mask;
      sfor<40>([&](auto nc) AI {
        constexpr int n = nc.value, u = n / 5, qt = n % 5;
        constexpr bool kd = MM && kcount(n + 1) != kcount(n) && !NO_KDMA;
        constexpr int j = kcount(n), jv = j >> 1, jh = j & 1;
        if constexpr (kd) set_m0<(X ^ 1) * 8192 + jh * 4096 + jv * (1024 - 128)>(slot);
        if constexpr (MM) { if (u == 0) mfma_zero(S[X ^ 1][qt], kf[u & 3], Q[u][qt]); else mfma_acc(S[X ^ 1][qt], kf[u & 3], Q[u][qt]); }
        if constexpr (kd) dma_m0<128 * jv, (KV_NT & 1) != 0>(rK, jh ? vK1 : vK0, so);
        SBAR();
        if constexpr (MM && qt == 0) {
          if constexpr (u == 1 || u == 3) wait_vm<8 + 2 * (3 - (u + 3) / 2) + kcount(n + 1)>();
          if constexpr (u == 5 || u == 7) wait_vm<2 * (3 - (u - 5) / 2) + kcount(n + 1)>();
          kf[(u + 3) & 3] = rdK(u < 5 ? X ^ 1 : X, (u + 3) & 7);
        }
        if (n == 1) fl = *(const LAS u32*)(fla + (X ^ 1) * 16);
        if (MM && n == 2) {
          so = (s < 13 ? tile_off((s + 3) & 15) : (u32)(SK * DH * 2)) + 16 * w * 512;
          asm volatile("" : "+s"(so));
        }
        if constexpr (n >= 1 && n < 31) {
          constexpr int c = (n - 1) / 6, k = (n - 1) % 6;
          if (k == 0) { t[0] = fmaf(S[X][c][0], SCL, -mref[c]); t[1] = fmaf(S[X][c][1], SCL, -mref[c]); asm volatile("" : "+v"(t[0])); asm volatile("" : "+v"(t[1])); }
          if (k == 1) { t[2] = fmaf(S[X][c][2], SCL, -mref[c]); t[0] = ex2(t[0]); t[3] = fmaf(S[X][c][3], SCL, -mref[c]); asm volatile("" : "+v"(t[2])); asm volatile("" : "+v"(t[3])); }
          if (k == 2) { t[1] = ex2(t[1]); t[2] = ex2(t[2]); }
          if (k == 3) { t[3] = ex2(t[3]); pkd[0] = pk(t[0], t[1]); t[0] = fadd(t[0], t[1]); }
          if (k == 4) { pkd[1] = pk(t[2], t[3]); t[2] = fadd(t[2], t[3]); }
          if (k == 5) { if (NO_WR) asm volatile("" :: "v"(pkd)); else *(LAS u32x2*)(pw + X * 16384 + c * 2048) = pkd; ps[c] = fadd(t[0], t[2]); }
        }
        if (n == 26) { asm volatile("" : "+v"(fl)); chk = max3(ps[0], ps[1], __uint_as_float(fl)); }
        if (n == 28) { chk = max3(chk, ps[2], ps[3]); if (!NO_WR || s < 2) *(LAS u32*)(flw + X * 16) = zero; }
        if (n == 31) chk = max2(chk, ps[4]);
        if (n == 32) { mask = __builtin_amdgcn_ballot_w64(chk > thr); asm volatile("" : "+s"(mask)); }
        if (n == 33) {
          if (__builtin_expect(mask != 0, 0)) {
            const bool ad = __any(fl != 0), s0 = s == 0;
            if (ad) adopt(X ^ 1, s == 1);
            float mm = -__builtin_inff(), mn = __builtin_inff();
#pragma unroll
            for (int c = 0; c < 5; ++c) {
              const float m = fmaf(fmaxf(fmaxf(S[X][c][0], S[X][c][1]), fmaxf(S[X][c][2], S[X][c][3])), SCL, -mref[c]);
              mm = fmaxf(mm, m); mn = fminf(mn, m);
            }
            const bool move = __any(mm > LTHR || (s0 && mn < -LTHR));
            if (move || ad) {
              const u32 q = fresh_lane() & 15;
#pragma unroll
              for (int c = 0; c < 5; ++c) {
                float d = __builtin_ceilf(xmax(fmaf(fmaxf(fmaxf(S[X][c][0], S[X][c][1]), fmaxf(S[X][c][2], S[X][c][3])), SCL, -mref[c])));
                d = move ? (s0 ? d : fmaxf(d, 0.f)) : 0.f;
                l[c] *= ex2(fminf(-d, 0.f)); mref[c] += d;
                const float p0 = ex2(fmaf(S[X][c][0], SCL, -mref[c])), p1 = ex2(fmaf(S[X][c][1], SCL, -mref[c]));
                const float p2 = ex2(fmaf(S[X][c][2], SCL, -mref[c])), p3 = ex2(fmaf(S[X][c][3], SCL, -mref[c]));
                *(LAS u32x2*)(pw + X * 16384 + c * 2048) = u32x2{pk(p0, p1), pk(p2, p3)};
                ps[c] = (p0 + p1) + (p2 + p3);
                *(LAS float*)(L_MX + X * 1280 + (c * 16 + q) * 16 + w * 4) = mref[c];
                asm volatile("" : "+v"(ps[c]), "+v"(l[c]), "+v"(mref[c]));
                SBAR();
              }
              if (move) { u32 inf = 0x7f800000u; asm volatile("" : "+v"(inf)); *(LAS u32*)(flw + X * 16) = inf; }
            }
            thr = PTHR;
          }
        }
        if (n == 34) { l[0] = fadd(l[0], ps[0]); l[1] = fadd(l[1], ps[1]); }
        if (n == 36) { l[2] = fadd(l[2], ps[2]); l[3] = fadd(l[3], ps[3]); }
        if (n == 37) l[4] = fadd(l[4], ps[4]);
        SBAR();
      });
      if constexpr (MM) asm volatile("s_waitcnt lgkmcnt(1)\n\ts_barrier" ::: "memory");
      else bar_lds();
    };
    using I0 = std::integral_constant<int, 0>; using I1 = std::integral_constant<int, 1>;
    step(I0{}, I1{}, 0);
    ST(9); STR(41);
#pragma unroll 1
    for (u32 s = 1; s < 15; s += 2) {
      step(I1{}, I1{}, s); step(I0{}, I1{}, s + 1);
    }
    ST(10); STR(42);
    wait_vm<0>();
    asm volatile("s_nop 7" ::: "memory");
    step(I1{}, I0{}, 15);
    if (__any(*(const LAS u32*)(fla + 16) != 0)) adopt(1, false);
    { const u32 q = fresh_lane() & 15;
#pragma unroll
      for (int qt = 0; qt < 5; ++qt) *(LAS float*)(L_LS + (qt * 16 + q) * 16 + w * 4) = xsum(l[qt]);
    }
    init_rv(DM * 2);
    if (w < 3) { issue(rWo, wo_soff(0), slot); issue(rWo, wo_soff(1), slot + 8192); }
    bar_lds();
    init_ab();
    wait_vm<12>();
    readA(A0, ab, 0);
    zero_acc();
    ST(58);
    bar();
    ST(59);
  } else {
    const rsrc_t rWq = mkrsrc(wq + head * DH, (DM * HQ * DH - head * DH) * 2);
    auto wq_soff = [&](u32 vp) AI { u32 v = vp + rot7; v = v >= 7 ? v - 7 : v; return v * 64 * (HQ * DH * 2) + w * 128; };
    init_rv(HQ * DH * 2);
    issue(rWq, wq_soff(0), slot);
    init_ab();
    zero_acc();
    SBAR();
    issue(rWq, wq_soff(1), slot + 8192);
    const rsrc_t rV = mkrsrc(vc + (size_t)(b * HKV + kvh) * SK * DH, KV_BYTES);
    const u32 wa = ((g >> 1) * 16 + q) * 16 + (g & 1) * 8;
    wait_vm<12>();
    readA(A0, ab, 0);
    {
      u32 xb = L_X + (q >> 3) * 1024 + (q & 7) * 128 + 16 * (g ^ (q & 6));
      bar();
      ST(1);
      readB(B0, xb);
      auto mid = [&]() AI { pin5(B1); bar();   xb += 10240; return xb; };
#pragma unroll 1
      for (u32 vp = 0; vp < 5; ++vp) gstep<true, false, 9, 9>(acc, A0, B0, B1, ab, slot, xb ^ 64, rWq, wq_soff(vp + 2), rv, mid, NoEp{});
      ST(2);
      gstep<false, false, 8, 4>(acc, A0, B0, B1, ab, slot, xb ^ 64, rWq, 0, rv, mid, NoEp{});
      ST(3);
      const u32 qa = slot + wa, qa2 = L_P + 2048 * w + wa;
      gstep<false, true, 0, 0>(acc, A0, B0, B1, ab, slot, xb ^ 64, rWq, 0, rv, [&]() AI { return 0u; }, [&](auto dtc, auto qtc) AI {
        constexpr int dt = dtc.value, qt = qtc.value;
        const f32x4 v = acc[dt][qt];
        *(LAS u32x2*)((qt < 4 ? qa + qt * 2048 : qa2) + (dt >> 1) * 1024 + (dt & 1) * 512) = u32x2{pk(v[0], v[1]), pk(v[2], v[3])};
      });
      ST(4);
      bar_lds();
      init_rv(512);
      issue(rV, tile_off(0) + w * 128, slot);
      zero_acc();
      bar();
      issue(rV, tile_off(1) + w * 128, slot ^ 8192u);
      wait_vm<12>();
      readA(A0, ab, 0);
      ST(5);
    }
    float mref[5];
#pragma unroll
    for (int qt = 0; qt < 5; ++qt) mref[qt] = 0.f;
    u32 pb = L_P + lane * 16;
    u32 fla = L_FLAG + (lane & 3) * 4;
    bar();
    ST(6);
    u32 fl = *(const LAS u32*)fla;
    readB(B0, pb);
    rsrc_t rr = rV;
    float f1[5];
    bool rare;
    auto pre = [&](u32 s) AI {
      rare = __any(fl != 0);
      if (__builtin_expect(rare, 0)) {
        const u32x4 f4 = ldu4(L_FLAG + (s & 1) * 16);
#pragma unroll
        for (int qt = 0; qt < 5; ++qt) {
          const f32x4 m4 = ldf4(L_MX + (s & 1) * 1280 + (qt * 16 + q) * 16);
          float r[4];
#pragma unroll
          for (int i = 0; i < 4; ++i) r[i] = f4[i] ? m4[i] : mref[qt];
          const float M = fmaxf(fmaxf(r[0], r[1]), fmaxf(r[2], r[3]));
          const float f = ex2(fminf(mref[qt] - M, 0.f));
#pragma unroll
          for (int dt = 0; dt < 4; ++dt) acc[dt][qt] *= f;
          B0[qt] = scale8(B0[qt], ex2(((g >> 1) ? r[1] : r[0]) - M));
          f1[qt] = ex2(((g >> 1) ? r[3] : r[2]) - M);
          mref[qt] = M;
        }
      }
    };
    auto mid = [&]() AI {
      pb ^= 16384u; fla ^= 16u;
      pin5(B1);
      if (__builtin_expect(rare, 0)) {
#pragma unroll
        for (int qt = 0; qt < 5; ++qt) B1[qt] = scale8(B1[qt], f1[qt]);
      }
      bar();
    };
#pragma unroll 1
    for (u32 s = 0; s < 15; ++s) {
      if (NO_PV) { bar(); continue; }
      u32 rsoff = tile_off((s + 2) & 15) + w * 128;
      if (__builtin_expect(s == 14, 0)) { rr = rWo; rsoff = wo_soff(0); init_rv(DM * 2); }
      pre(s);
      gstep<true, false, 9, 9, (KV_NT & 2) != 0>(acc, A0, B0, B1, ab, slot, pb + 1024, rr, rsoff, rv, [&]() AI { mid(); fl = *(const LAS u32*)fla; return pb; }, NoEp{});
    }
    ST(56);
    pre(15);
    float inv[5];
    const u32 oa = L_O + w * 10240 + wa;
    gstep<true, true, 9, 0>(acc, A0, B0, B1, ab, slot, pb + 1024, rWo, wo_soff(1), rv, [&]() AI {
      mid();
#pragma unroll
      for (int qt = 0; qt < 5; ++qt) { const f32x4 l4 = ldf4(L_LS + (qt * 16 + q) * 16); inv[qt] = __builtin_amdgcn_rcpf((l4[0] + l4[1]) + (l4[2] + l4[3])); }
      return 0u;
    }, [&](auto dtc, auto qtc) AI {
      constexpr int dt = dtc.value, qt = qtc.value;
      const f32x4 v = acc[dt][qt] * inv[qt];
      *(LAS u32x2*)(oa + qt * 2048 + (dt >> 1) * 1024 + (dt & 1) * 512) = u32x2{pk(v[0], v[1]), pk(v[2], v[3])};
    });
    wait_vm<12>();
    readA(A0, ab, 0);
    zero_acc();
    ST(58);
    asm volatile("s_waitcnt lgkmcnt(8)\n\ts_barrier" ::: "memory");
    ST(59);
  }
  if (wid != 3) {
    const u32 lane = fresh_lane();
    slot = wid < 4 ? L_KC + 16384 * w : L_VF + 16384 * w + 8192;
    auto ob = [&](u32 j) AI { return L_O + ((rot + j) & 3) * 10240 + lane * 16; };
    readB(B0, ob(0));
#pragma unroll 1
    for (u32 j = 0; j < 2; ++j)
      gstep<true, false, 9, 9>(acc, A0, B0, B1, ab, slot, ob(j) + 1024, rWo, wo_soff(j + 2), rv, [&]() AI { return ob(j + 1); }, NoEp{});
    gstep<false, false, 8, 4>(acc, A0, B0, B1, ab, slot, ob(2) + 1024, rWo, 0, rv, [&]() AI { return ob(3); }, NoEp{});
    ST(60);
    const __amdgpu_buffer_rsrc_t rP = __builtin_amdgcn_make_buffer_rsrc(ws + W_PART + (size_t)(grp * HQ + head) * PART_BYTES, (short)0, PART_BYTES, 0x00020000);
    const u32 po = nb * 10240 + lane * 16;
    gstep<false, true, 0, 0>(acc, A0, B0, B1, ab, slot, ob(3) + 1024, rWo, 0, rv, [&]() AI { return 0u; }, [&](auto dtc, auto qtc) AI {
      constexpr int dt = dtc.value, qt = qtc.value;
      if constexpr (dt & 1) {
        const f32x4 a = acc[dt - 1][qt], c = acc[dt][qt];
        __builtin_amdgcn_raw_buffer_store_b128(u32x4{pk(a[0], a[1]), pk(a[2], a[3]), pk(c[0], c[1]), pk(c[2], c[3])}, rP, po, (qt * 2 + (dt >> 1)) * 1024, 16);
      }
    });
    ST(61);
  }
  unsigned long long tag = 0;
  if (wid == 3) tag = __hip_atomic_load((gu64*)(ws + W_EPOCH + grp * 128), __ATOMIC_RELAXED, __HIP_MEMORY_SCOPE_AGENT) + 1;
  wait_vm<0>();
  ST(62);
  bar();
  {
    const u32 lane = fresh_lane();
    gu64* flags = (gu64*)(ws + W_FLAG + grp * HQ * 128);
    if (wid == 3) {
      if (lane == 0) __hip_atomic_store(flags + head * 16, tag, __ATOMIC_RELAXED, __HIP_MEMORY_SCOPE_AGENT);
#pragma unroll 1
      for (u32 spins = 0; spins < (1u << 24); ++spins) {
        const unsigned long long f = __hip_atomic_load(flags + (lane & 15) * 16, __ATOMIC_RELAXED, __HIP_MEMORY_SCOPE_AGENT);
        if (__all(f == tag)) break;
        __builtin_amdgcn_s_sleep(1);
      }
      if (head == 0 && lane == 0) __hip_atomic_store((gu64*)(ws + W_EPOCH + grp * 128), tag, __ATOMIC_RELAXED, __HIP_MEMORY_SCOPE_AGENT);
    }
    bar();
    ST(63);
    const u32 tid = wid * 64 + lane;
    if (tid < 280) {
      const __amdgpu_buffer_rsrc_t rG = __builtin_amdgcn_make_buffer_rsrc(ws + W_PART + (size_t)grp * HQ * PART_BYTES, (short)0, HQ * PART_BYTES, 0x00020000);
      const u32 t = head * 280 + tid, ln = t & 63, unit = t >> 6;
      u32x4 v[16];
#pragma unroll
      for (int h = 0; h < 16; ++h) v[h] = __builtin_amdgcn_raw_buffer_load_b128(rG, t * 16, h * PART_BYTES, 16);
      float a[8] = {0.f, 0.f, 0.f, 0.f, 0.f, 0.f, 0.f, 0.f};
#pragma unroll
      for (int h = 0; h < 16; ++h)
#pragma unroll
        for (int i = 0; i < 4; ++i) { a[2 * i] += __uint_as_float(v[h][i] << 16); a[2 * i + 1] += __uint_as_float(v[h][i] & 0xffff0000u); }
      const u32 ub = unit / 10, r = unit - 10 * ub;
      u16* o = out + (size_t)(grp * ROWS + (r >> 1) * 16 + (ln & 15)) * DM + ub * 64 + (r & 1) * 32 + 4 * (ln >> 4);
      *(u32x2*)o = u32x2{pk(a[0], a[1]), pk(a[2], a[3])};
      *(u32x2*)(o + 16) = u32x2{pk(a[4], a[5]), pk(a[6], a[7])};
    }
  }
  ST_OUT();
}

extern "C" void kernel_launch(void* const* d_in, const int* in_sizes, int n_in, void* d_out, int out_size, void* d_ws, size_t ws_size,
                              hipStream_t stream) {
  (void)in_sizes; (void)n_in; (void)out_size; (void)ws_size;
  (void)hipFuncSetAttribute((const void*)fused_attn, hipFuncAttributeMaxDynamicSharedMemorySize, (int)L_END);
  hipLaunchKernelGGL(fused_attn, dim3(256), dim3(512), L_END, stream, (const u16*)d_in[0], (const u16*)d_in[1], (const u16*)d_in[2],
                     (const u16*)d_in[3], (const u16*)d_in[4], (char*)d_ws, (u16*)d_out);
}
```

```cpp
#include <hip/hip_runtime.h>
#include <stdint.h>
#include <utility>

typedef unsigned int u32;
typedef unsigned short u16;
typedef __bf16 bf16x8_t __attribute__((ext_vector_type(8)));
typedef __bf16 bf16x2 __attribute__((ext_vector_type(2)));
typedef short s16x4 __attribute__((ext_vector_type(4)));
typedef float f32x4 __attribute__((ext_vector_type(4)));
typedef float f32x2 __attribute__((ext_vector_type(2)));
typedef u32 u32x2 __attribute__((ext_vector_type(2)));
typedef u32 u32x4 __attribute__((ext_vector_type(4)));
typedef u32x4 rsrc_t;
typedef u32x4 bf16x8;
#define LAS __attribute__((address_space(3)))
#define DI __device__ __forceinline__
#define AI __attribute__((always_inline))
#define SBAR() __builtin_amdgcn_sched_barrier(0)

template <class F, int... Is> DI void sfor_(F&& f, std::integer_sequence<int, Is...>) { (f(std::integral_constant<int, Is>{}), ...); }
template <int N, class F> DI void sfor(F&& f) { sfor_(f, std::make_integer_sequence<int, N>{}); }

constexpr int SQ = 160, DM = 448, HQ = 16, HKV = 8, SK = 1024, DH = 256;
constexpr int ROWS = 80;
constexpr float SCL = 0.0625f * 1.4426950408889634f;
constexpr float PTHR = 1048576.0f;
constexpr float LTHR = 18.0f;

constexpr u32 L_VF = 0;
constexpr u32 L_KC = 65536;
constexpr u32 L_X = 98304;
constexpr u32 L_P = 131072;
constexpr u32 L_MX = 141312;
constexpr u32 L_FLAG = 143872;
constexpr u32 L_O = 131072, L_O3 = 49152;
constexpr u32 L_LS = 161792;
constexpr u32 L_SYNC = 163072;
constexpr u32 L_DUMP = 163328;
constexpr u32 L_END = 163840;
constexpr u32 W_EPOCH = 0, W_FLAG = 16 * 128, W_PART = 131072, PART_BYTES = 70 * 1024;
typedef __attribute__((address_space(1))) unsigned long long gu64;

#define KV_NT 0
#define KV_ORDER 1
template <bool NT = false> DI void dma(rsrc_t r, u32 voff, u32 soff, u32 ldsaddr) {
  if (NT) asm volatile("s_mov_b32 m0, %0\n\ts_nop 0\n\tbuffer_load_dwordx4 %1, %2, %3 offen nt lds" ::"s"(ldsaddr), "v"(voff), "s"(r), "s"(soff) : "memory");
  else asm volatile("s_mov_b32 m0, %0\n\ts_nop 0\n\tbuffer_load_dwordx4 %1, %2, %3 offen lds" ::"s"(ldsaddr), "v"(voff), "s"(r), "s"(soff) : "memory");
}
template <int LOFF> DI void set_m0(u32 lds) { asm volatile("s_add_u32 m0, %0, %1" ::"s"(lds), "i"(LOFF) : "memory", "scc"); }
template <int IMM, bool NT = false> DI void dma_m0(rsrc_t r, u32 voff, u32 soff) {
  if (NT) asm volatile("buffer_load_dwordx4 %0, %1, %2 offen offset:%3 nt lds" ::"v"(voff), "s"(r), "s"(soff), "i"(IMM) : "memory");
  else asm volatile("buffer_load_dwordx4 %0, %1, %2 offen offset:%3 lds" ::"v"(voff), "s"(r), "s"(soff), "i"(IMM) : "memory");
}
DI void mfma_acc(f32x4& c, const bf16x8& a, const bf16x8& b) { asm volatile("v_mfma_f32_16x16x32_bf16 %0, %1, %2, %0" : "+v"(c) : "v"(a), "v"(b)); }
DI void mfma_zero(f32x4& d, const bf16x8& a, const bf16x8& b) { asm volatile("v_mfma_f32_16x16x32_bf16 %0, %1, %2, 0" : "+v"(d) : "v"(a), "v"(b)); }
DI void pin4(const bf16x8 (&A)[4]) { asm volatile("" ::"v"(A[0]), "v"(A[1]), "v"(A[2]), "v"(A[3]) : "memory"); }
DI void pin5(const bf16x8 (&A)[5]) { asm volatile("" ::"v"(A[0]), "v"(A[1]), "v"(A[2]), "v"(A[3]), "v"(A[4]) : "memory"); }
template <int N> DI void wait_vm() { asm volatile("s_waitcnt vmcnt(%0)" ::"i"(N) : "memory"); }
DI void bar() { asm volatile("s_barrier" ::: "memory"); }
DI void bar_lds() { asm volatile("s_waitcnt lgkmcnt(0)\n\ts_barrier" ::: "memory"); }
DI bf16x8 ld128(u32 a) { return *(const LAS bf16x8*)a; }
DI f32x4 ldf4(u32 a) { return *(const LAS f32x4*)a; }
DI u32x4 ldu4(u32 a) { return *(const LAS u32x4*)a; }
DI u32x2 ldtr1(u32 a) { return __builtin_bit_cast(u32x2, __builtin_amdgcn_ds_read_tr16_b64_v4i16((LAS s16x4*)a)); }
DI u32 pk(float lo, float hi) { f32x2 v = {lo, hi}; return __builtin_bit_cast(u32, __builtin_convertvector(v, bf16x2)); }
DI f32x4 mfma(bf16x8 a, bf16x8 b, f32x4 c) { return __builtin_amdgcn_mfma_f32_16x16x32_bf16(__builtin_bit_cast(bf16x8_t, a), __builtin_bit_cast(bf16x8_t, b), c, 0, 0, 0); }
DI rsrc_t mkrsrc(const void* p, u32 bytes) { const uint64_t a = (uint64_t)p; return rsrc_t{(u32)a, (u32)(a >> 32) & 0xffffu, bytes, 0x00020000u}; }
DI u32 fresh_lane() { u32 l; asm volatile("v_mbcnt_lo_u32_b32 %0, -1, 0\n\tv_mbcnt_hi_u32_b32 %0, -1, %0" : "=v"(l)); return l; }
DI float ex2(float x) { return __builtin_amdgcn_exp2f(x); }
DI float max3(float a, float b, float c) { float r; asm("v_max3_f32 %0, %1, %2, %3" : "=v"(r) : "v"(a), "v"(b), "v"(c)); return r; }
DI float max2(float a, float b) { float r; asm("v_max_f32_e32 %0, %1, %2" : "=v"(r) : "v"(a), "v"(b)); return r; }
DI float fadd(float a, float b) { float r; asm("v_add_f32_e32 %0, %1, %2" : "=v"(r) : "v"(a), "v"(b)); return r; }
DI float xmax(float v) {
  auto a = __builtin_amdgcn_permlane16_swap(__float_as_uint(v), __float_as_uint(v), false, false);
  v = fmaxf(__uint_as_float(a[0]), __uint_as_float(a[1]));
  auto b = __builtin_amdgcn_permlane32_swap(__float_as_uint(v), __float_as_uint(v), false, false);
  return fmaxf(__uint_as_float(b[0]), __uint_as_float(b[1]));
}
DI float xsum(float v) {
  auto a = __builtin_amdgcn_permlane16_swap(__float_as_uint(v), __float_as_uint(v), false, false);
  v = __uint_as_float(a[0]) + __uint_as_float(a[1]);
  auto b = __builtin_amdgcn_permlane32_swap(__float_as_uint(v), __float_as_uint(v), false, false);
  return __uint_as_float(b[0]) + __uint_as_float(b[1]);
}
DI bf16x8 scale8(bf16x8 u, float f) {
#pragma unroll
  for (int i = 0; i < 4; ++i) {
    const float lo = __uint_as_float(u[i] << 16) * f, hi = __uint_as_float(u[i] & 0xffff0000u) * f;
    u32 r; asm("v_cvt_pk_bf16_f32 %0, %1, %2" : "=v"(r) : "v"(lo), "v"(hi));
    u[i] = r;
  }
  asm volatile("s_nop 1" : "+v"(u));
  return u;
}

#define ST(i)
#define STR(i)
#define ST_OUT()
#define NO_PV 0
#define WQ_TOUCH 1
#define EXP_NOWAIT 0
#define WPOS 6, 12, 18, 24, 30
#define FPOS 28
#define K1_LATE 0
#define START_SLEEP 6
constexpr u32 CODE_LINES = 8;
#define NO_WR 0
#define KV_BYTES (SK * DH * 2)
#define NO_KDMA 0
#define VDMA 1

DI void readA1(bf16x8 (&A)[4], const u32 (&ab)[4], int e, int i) {
  const int dt = i >> 1;
  const u32x2 v = (i & 1) ? ldtr1(ab[dt ^ 2] + 4096 * e + 512) : ldtr1(ab[dt] + 4096 * e);
  if (i & 1) { A[dt][2] = v[0]; A[dt][3] = v[1]; } else { A[dt][0] = v[0]; A[dt][1] = v[1]; }
}
DI void readA(bf16x8 (&A)[4], const u32 (&ab)[4], int e) {
#pragma unroll
  for (int i = 0; i < 8; ++i) readA1(A, ab, e, i);
}
DI void readB(bf16x8 (&B)[5], u32 bb) {
#pragma unroll
  for (int qt = 0; qt < 5; ++qt) B[qt] = ld128(bb + qt * 2048);
}
constexpr int vpos[4] = {3, 8, 13, 18};
template <bool RF, bool LAST, int W1, int W0, bool NT = false, class Mid, class Ep>
DI void gstep(f32x4 (&acc)[4][5], bf16x8 (&A0)[4], bf16x8 (&B0)[5], bf16x8 (&B1)[5], u32 (&ab)[4], u32& slot, u32 b1,
              rsrc_t rr, u32 rsoff, const u32 (&rv)[8], Mid mid, Ep ep) {
  bf16x8 A1[4];
  sfor<20>([&](auto nc) AI {
    constexpr int n = nc.value, j = n == vpos[0] ? 0 : n == vpos[1] ? 1 : n == vpos[2] ? 2 : n == vpos[3] ? 3 : -1;
    if constexpr (RF && j >= 0) { if (j == 0) pin4(A0); set_m0<1024 * (j < 0 ? 0 : j)>(slot); SBAR(); }
    acc[n / 5][n % 5] = mfma(A0[n / 5], B0[n % 5], acc[n / 5][n % 5]);
    SBAR();
    if constexpr (RF && j >= 0 && VDMA) dma_m0<0, NT>(rr, rv[j < 0 ? 0 : j], rsoff);
    if constexpr (RF && VDMA == 2 && n % 5 == 0 && n > 0) dma_m0<0>(rr, rv[n / 5 - 1], rsoff);
    if (n < 5) B1[n % 5] = ld128(b1 + n * 2048);
    if (n == 5) wait_vm<W1>();
    if (n >= 5 && n < 13) readA1(A1, ab, 1, n - 5);
    if (n == 16) { ab[0] ^= 8192u; ab[1] ^= 8192u; asm volatile("" : "+v"(ab[0]), "+v"(ab[1])); }
    if (n == 17) { ab[2] ^= 8192u; ab[3] ^= 8192u; asm volatile("" : "+v"(ab[2]), "+v"(ab[3])); }
    SBAR();
  });
  const u32 b0 = mid();
  SBAR();
  sfor<20>([&](auto nc) AI {
    constexpr int n = nc.value, j = n == vpos[0] ? 4 : n == vpos[1] ? 5 : n == vpos[2] ? 6 : n == vpos[3] ? 7 : -1;
    constexpr int dt = LAST ? n % 4 : n / 5, qt = LAST ? n / 4 : n % 5;
    if constexpr (RF && j >= 0) { if (j == 4) pin4(A1); set_m0<1024 * (j < 0 ? 0 : j)>(slot); SBAR(); }
    acc[dt][qt] = mfma(A1[dt], B1[qt], acc[dt][qt]);
    SBAR();
    if constexpr (RF && j >= 0 && VDMA) dma_m0<0, NT>(rr, rv[j < 0 ? 0 : j], rsoff);
    if constexpr (RF && VDMA == 2 && n % 5 == 0) dma_m0<0>(rr, rv[n / 5 + 3], rsoff);
    if constexpr (RF && VDMA == 2 && n == 19) dma_m0<0>(rr, rv[7], rsoff);
    if constexpr (!LAST) {
      if (n < 5) B0[n % 5] = ld128(b0 + n * 2048);
      if (n == 5) wait_vm<W0>();
      if (n >= 5 && n < 13) readA1(A0, ab, 0, n - 5);
    } else {
      if constexpr (n >= 4) ep(std::integral_constant<int, 2>{}, std::integral_constant<int, n - 4>{});
      if constexpr (n >= 3) ep(std::integral_constant<int, 1>{}, std::integral_constant<int, n - 3>{});
      if constexpr (n >= 2) ep(std::integral_constant<int, 0>{}, std::integral_constant<int, n - 2>{});
    }
    SBAR();
  });
  if constexpr (LAST) sfor<4>([&](auto nc) AI {
    constexpr int n = 20 + nc.value;
    ep(std::integral_constant<int, 2>{}, std::integral_constant<int, n - 4>{});
    if constexpr (n - 3 < 20) ep(std::integral_constant<int, 1>{}, std::integral_constant<int, n - 3>{});
    if constexpr (n - 2 < 20) ep(std::integral_constant<int, 0>{}, std::integral_constant<int, n - 2>{});
    SBAR();
  });
  slot ^= 8192u;
}
struct NoEp { template <class A, class B> DI void operator()(A, B) const {} };

constexpr int kpos[8] = {6, 11, 16, 21, 26, 31, 36, 38};
constexpr int kcount(int n) { int c = 0; for (int j = 0; j < 8; ++j) c += kpos[j] < n; return c; }
constexpr uint64_t kv_seq(u32 cu) {
  if (KV_ORDER == 1) { uint64_t t = 0; for (u32 i = 0; i < 16; ++i) { const u32 d = (cu - i) & 3, g = d == 0 ? 0 : (d == 1 ? 2 : 3); t |= (uint64_t)((i - g) & 15) << (4 * i); } return t; }
  return KV_ORDER == 0 ? 0xfedcba9876543210ull : cu == 0 ? 0xfedbac9768532410ull : cu == 1 ? 0xfecbda8796425301ull : cu == 2 ? 0xfdceb98a75461032ull : 0xedfca9b865740123ull;
}
DI void touch(rsrc_t r, u32 voff, u32 soff) {
  asm volatile("s_mov_b32 m0, %0\n\ts_nop 0\n\tbuffer_load_dword %1, %2, %3 offen lds" ::"s"(L_DUMP), "v"(voff), "s"(r), "s"(soff) : "memory");
}

__global__ __launch_bounds__(512, 2) void fused_attn(const u16* __restrict__ x, const u16* __restrict__ kc, const u16* __restrict__ vc,
                                                     const u16* __restrict__ wq, const u16* __restrict__ wo, char* __restrict__ ws, u16* __restrict__ out) {
  uint64_t pc0; asm volatile("s_getpc_b64 %0" : "=s"(pc0));
  const u32 lane = threadIdx.x & 63, wid = __builtin_amdgcn_readfirstlane(threadIdx.x >> 6), w = wid & 3;
  const u32 blk = blockIdx.x, kvh = blk & 7, idx = blk >> 3, b = idx >> 2, cu = idx & 3, head = 2 * kvh + (cu >> 1), half = cu & 1;
  ST(0); STR(40);
  const u32 q = lane & 15, g = lane >> 4;
  const u32 lr = lane >> 3, lc = (lane & 7) ^ (lr & 6);
  const u32 rot = 2 * b + half, rot7 = (rot + 4 * (cu >> 1)) % 7;
  const uint64_t tseq = cu == 0 ? kv_seq(0) : cu == 1 ? kv_seq(1) : cu == 2 ? kv_seq(2) : kv_seq(3);
  auto tile_off = [&](u32 s) AI { return ((u32)(tseq >> (4 * s)) & 15) * (64 * 512); };
  const u32 grp = 2 * b + half;
  auto wo_soff = [&](u32 j) AI { return j * 64 * (DM * 2) + wid * 128; };
  bf16x8 WA[8][4];

  f32x4 acc[4][5];
  bf16x8 A0[4], B0[5], B1[5];
  u32 ab[4], rv[8], slot = wid < 4 ? L_KC + 8192 * w : L_VF + 16384 * w;
  auto init_ab = [&](u32 base, u32 pstride) AI {
    const u32 lane = fresh_lane(), g = lane >> 4;
    const u32 q4 = (lane & 15) >> 2, p = lane & 3, f0 = (q4 & 2) ^ ((g & 1) << 2);
#pragma unroll
    for (int i = 0; i < 4; ++i) ab[i] = base + g * pstride + q4 * 128 + 16 * (((2 * i) ^ f0) | (p >> 1)) + 8 * (p & 1);
  };
  auto init_rv = [&](u32 stride) AI {
    const u32 lane = fresh_lane(), lr = lane >> 3, lc = (lane & 7) ^ (lr & 6);
#pragma unroll
    for (int i = 0; i < 8; ++i) rv[i] = (lr + 8 * i) * stride + ((lc * 16) ^ ((i & 1) * 64));
  };
  auto issue = [&](rsrc_t rr, u32 so, u32 dst) AI {
#pragma unroll
    for (int i = 0; i < 8; ++i) dma(rr, rv[i], so, dst + i * 1024);
  };
  auto zero_acc = [&]() AI {
#pragma unroll
    for (int dt = 0; dt < 4; ++dt)
#pragma unroll
      for (int qt = 0; qt < 5; ++qt) { acc[dt][qt] = f32x4{0.f, 0.f, 0.f, 0.f}; asm volatile("" : "+v"(acc[dt][qt])); }
  };
  auto readWA = [&](int k, u32 off) AI {
#pragma unroll
    for (int dt = 0; dt < 4; ++dt) { const u32x2 lo = ldtr1(ab[dt] + off), hi = ldtr1(ab[dt ^ 2] + off + 512); WA[k][dt] = bf16x8{lo[0], lo[1], hi[0], hi[1]}; }
  };
  const rsrc_t rWq = mkrsrc(wq + head * DH, (DM * HQ * DH - head * DH) * 2);
  auto wq_soff = [&](u32 vp) AI { u32 v = vp + rot7; v = v >= 7 ? v - 7 : v; return v * 64 * (HQ * DH * 2) + w * 128; };
  const rsrc_t rX = mkrsrc(x + (size_t)(b * SQ + half * ROWS) * DM, ROWS * DM * 2);
  const u32 voffX = lr * (DM * 2) + lc * 16;
  u32 xv = rot7, xdst = L_X + 5 * 10240;
  auto issueX = [&]() AI {
    dma(rX, voffX, w * 8 * (DM * 2) + xv * 128, xdst + w * 1024);
    dma(rX, voffX, (w + 4) * 8 * (DM * 2) + xv * 128, xdst + (w + 4) * 1024);
    if (w < 2) dma(rX, voffX, (w + 8) * 8 * (DM * 2) + xv * 128, xdst + (w + 8) * 1024);
    xv = xv == 6 ? 0 : xv + 1; xdst = xdst == L_X + 5 * 10240 ? L_X : xdst + 10240;
  };
  if (wid < 4) {
    issueX();
    if (w == 3) {
      const u32 ln = lane < CODE_LINES ? lane : 0;
      touch(mkrsrc((const void*)(pc0 & ~(uint64_t)127), 32 * CODE_LINES * 128), ln * 32 * 128, idx * 128);
    }
    __builtin_amdgcn_s_sleep(START_SLEEP);
    issueX();
  } else {
    init_rv(HQ * DH * 2);
    issue(rWq, wq_soff(0), slot);
  }
  const rsrc_t rWo = mkrsrc(wo + (size_t)head * DH * DM, DH * DM * 2);

  if (wid < 4) {
    rsrc_t rK = mkrsrc(kc + (size_t)(b * HKV + kvh) * SK * DH, KV_BYTES);
    u32 vK0 = lr * 512 + lc * 16, vK1 = vK0 + 4096;
    auto issueK = [&](u32 j, u32 ks) AI {
      const u32 so = tile_off(j) + 16 * w * 512;
#pragma unroll
      for (int v = 0; v < 4; ++v) { dma<(KV_NT & 1) != 0>(rK, vK0, so + 128 * v, ks + v * 1024); dma<(KV_NT & 1) != 0>(rK, vK1, so + 128 * v, ks + 4096 + v * 1024); }
    };
    {
#pragma unroll 1
      for (u32 vp = 0; vp < 5; ++vp) {
        if (w < 2) wait_vm<3>(); else wait_vm<2>();
        bar();
        issueX();
      }
      wait_vm<0>();
      if (w == 3) {
        const unsigned long long tag = __hip_atomic_load((gu64*)(ws + W_EPOCH + grp * 128), __ATOMIC_RELAXED, __HIP_MEMORY_SCOPE_AGENT) + 1;
        *(LAS u32x4*)L_SYNC = u32x4{0, 0, 0, 0};
        *(LAS unsigned long long*)(L_SYNC + 16) = tag;
      }
      ST(1);
      bar();
      bar();
    }
    ST(2);
    issueK(0, slot);
    if (!K1_LATE) issueK(1, slot + 32768);
    u32 kb0 = slot + (q >> 3) * 4096 + (q & 7) * 128 + 16 * (g ^ (q & 6)), kb1 = kb0 ^ 64;
    u32 pw = L_P + (((2 * w + (g >> 1)) & 3) * 16 + q) * 16 + (g & 1) * 8 + (w >> 1) * 1024;
    if (NO_WR == 3) pw = L_P + w * 512 + lane * 8;
    asm volatile("" : "+v"(kb0), "+v"(kb1), "+v"(pw));
    float mref[5], l[5];
#pragma unroll
    for (int qt = 0; qt < 5; ++qt) { mref[qt] = 0.f; l[qt] = 0.f; }
    f32x4 S[2][5];
    bf16x8 kf[4];
    auto rdK = [&](int sl, int u) AI { return ld128(((u & 1) ? kb1 : kb0) + sl * 32768 + (u >> 1) * 1024); };
    ST(5);
    bar();
    if (K1_LATE) issueK(1, slot + 32768);
    ST(6);
    bf16x8 Q[8][5];
    auto rdQ = [&](int u, int qt) AI { Q[u][qt] = ld128(lane * 16 + (qt < 4 ? L_VF + 16384 * (u >> 1) + qt * 2048 : L_P + 2048 * (u >> 1)) + (u & 1) * 1024); };
    const u32 so2 = tile_off(2) + 16 * w * 512;
    wait_vm<8>();
    sfor<8>([&](auto uc) AI {
      constexpr int u = uc.value;
      if (u == 0) { kf[0] = rdK(0, 0); rdQ(0, 0); rdQ(0, 1); rdQ(0, 2); rdQ(0, 3); rdQ(0, 4); }
      if (u < 7) { kf[(u + 1) & 1] = rdK(0, u + 1); rdQ((u + 1) & 7, 0); rdQ((u + 1) & 7, 1); rdQ((u + 1) & 7, 2); rdQ((u + 1) & 7, 3); rdQ((u + 1) & 7, 4); }
#pragma unroll
      for (int qt = 0; qt < 5; ++qt) S[0][qt] = mfma(kf[u & 1], Q[u][qt], u == 0 ? f32x4{0.f, 0.f, 0.f, 0.f} : S[0][qt]);
      SBAR();
      if constexpr (u & 1) {
        asm volatile("" ::"v"(kf[1]) : "memory");
        dma<(KV_NT & 1) != 0>(rK, vK0, so2 + 64 * (u - 1), slot + (u >> 1) * 1024); dma<(KV_NT & 1) != 0>(rK, vK1, so2 + 64 * (u - 1), slot + 4096 + (u >> 1) * 1024);
      }
    });
#pragma unroll
    for (int qt = 0; qt < 5; ++qt) S[1][qt] = f32x4{0.f, 0.f, 0.f, 0.f};
    u32 fla = L_FLAG + (lane & 3) * 4;
    u32 flw = L_FLAG + w * 4;
    asm volatile("" : "+v"(flw), "+v"(fla));
    *(LAS u32*)(flw + 16) = 0;
    bar_lds();
    wait_vm<12>();
    kf[0] = rdK(1, 0); kf[1] = rdK(1, 1); kf[2] = rdK(1, 2);
    ST(7);

    auto adopt = [&](u32 par, bool s0) AI {
      const u32 q = fresh_lane() & 15;
      const u32x4 f4 = ldu4((flw & ~15u) + par * 16);
#pragma unroll
      for (int qt = 0; qt < 5; ++qt) {
        const f32x4 m4 = ldf4(L_MX + par * 1280 + (qt * 16 + q) * 16);
        const float own = mref[qt], base = s0 ? 0.f : own;
        float M = own;
#pragma unroll
        for (int i = 0; i < 4; ++i) M = fmaxf(M, f4[i] ? m4[i] : base);
        l[qt] *= ex2(own - M); mref[qt] = M;
        asm volatile("" : "+v"(l[qt]), "+v"(mref[qt]));
        SBAR();
      }
    };
    float t[4], ps[5];
    u32x2 pkd[3];
    float thr;
    { float mn = __builtin_inff();
#pragma unroll
      for (int c = 0; c < 5; ++c) mn = fminf(mn, fmaxf(fmaxf(S[0][c][0], S[0][c][1]), fmaxf(S[0][c][2], S[0][c][3])));
      thr = __any(mn * SCL < -LTHR) ? -1.f : PTHR;
    }
    const u32 zero = 0;
    auto step = [&](auto xc, auto mc, u32 s) AI {
      constexpr int X = xc.value; constexpr bool MM = mc.value;
      u32 fl, so; float chk; uint64_t mask;
      sfor<40>([&](auto nc) AI {
        constexpr int n = nc.value, u = n / 5, qt = n % 5;
        constexpr bool kd = MM && kcount(n + 1) != kcount(n) && !NO_KDMA;
        constexpr int j = kcount(n), jv = j >> 1, jh = j & 1;
        if constexpr (kd) set_m0<(X ^ 1) * 32768 + jh * 4096 + jv * (1024 - 128)>(slot);
        if constexpr (MM) { if (u == 0) mfma_zero(S[X ^ 1][qt], kf[u & 3], Q[u][qt]); else mfma_acc(S[X ^ 1][qt], kf[u & 3], Q[u][qt]); }
        if constexpr (kd) dma_m0<128 * jv, (KV_NT & 1) != 0>(rK, jh ? vK1 : vK0, so);
        SBAR();
        if constexpr (MM && qt == 0) {
          if constexpr (u == 1 || u == 3) wait_vm<8 + 2 * (3 - (u + 3) / 2) + kcount(n + 1)>();
          if constexpr (u == 5 || u == 7) wait_vm<2 * (3 - (u - 5) / 2) + kcount(n + 1)>();
          kf[(u + 3) & 3] = rdK(u < 5 ? X ^ 1 : X, (u + 3) & 7);
        }
        if (n == 1) fl = *(const LAS u32*)(fla + (X ^ 1) * 16);
        if (MM && n == 2) {
          so = tile_off((s + 3) & 15) + 16 * w * 512;
          if (__builtin_expect(s >= 13, 0)) {
            const u32 ln = fresh_lane(), r = ln >> 3;
            rK = rWo; so = ((s - 13) * 64 + 16 * w) * (DM * 2);
            vK0 = r * (DM * 2) + ((ln & 7) ^ (r & 6)) * 16; vK1 = (vK0 ^ 64u) + 8 * (DM * 2);
          }
          asm volatile("" : "+s"(so));
        }
        if constexpr (n >= 1 && n < 31) {
          constexpr int c = (n - 1) / 6, k = (n - 1) % 6;
          if (k == 0) { t[0] = fmaf(S[X][c][0], SCL, -mref[c]); t[1] = fmaf(S[X][c][1], SCL, -mref[c]); asm volatile("" : "+v"(t[0])); asm volatile("" : "+v"(t[1])); }
          if (k == 1) { t[2] = fmaf(S[X][c][2], SCL, -mref[c]); t[0] = ex2(t[0]); t[3] = fmaf(S[X][c][3], SCL, -mref[c]); asm volatile("" : "+v"(t[2])); asm volatile("" : "+v"(t[3])); }
          if (k == 2) { t[1] = ex2(t[1]); t[2] = ex2(t[2]); }
          if (k == 3) { t[3] = ex2(t[3]); pkd[c % 3][0] = pk(t[0], t[1]); t[0] = fadd(t[0], t[1]); }
          if (k == 4) { pkd[c % 3][1] = pk(t[2], t[3]); t[2] = fadd(t[2], t[3]); }
          if (k == 5) ps[c] = fadd(t[0], t[2]);
        }
        { constexpr int wpos[5] = {WPOS};
          sfor<5>([&](auto cc) AI { constexpr int c = cc.value; if constexpr (n == wpos[c]) *(LAS u32x2*)(pw + X * 16384 + c * 2048) = pkd[c % 3]; }); }
        if (n == 26) { asm volatile("" : "+v"(fl)); chk = max3(ps[0], ps[1], __uint_as_float(fl)); }
        if (n == 28) chk = max3(chk, ps[2], ps[3]);
        if (n == FPOS) *(LAS u32*)(flw + X * 16) = zero;
        if (n == 31) chk = max2(chk, ps[4]);
        if (n == 32) { mask = __builtin_amdgcn_ballot_w64(chk > thr); asm volatile("" : "+s"(mask)); }
        if (n == 33) {
          if (__builtin_expect(mask != 0, 0)) {
            const bool ad = __any(fl != 0), s0 = s == 0;
            if (ad) adopt(X ^ 1, s == 1);
            float mm = -__builtin_inff(), mn = __builtin_inff();
#pragma unroll
            for (int c = 0; c < 5; ++c) {
              const float m = fmaf(fmaxf(fmaxf(S[X][c][0], S[X][c][1]), fmaxf(S[X][c][2], S[X][c][3])), SCL, -mref[c]);
              mm = fmaxf(mm, m); mn = fminf(mn, m);
            }
            const bool move = __any(mm > LTHR || (s0 && mn < -LTHR));
            if (move || ad) {
              const u32 q = fresh_lane() & 15;
#pragma unroll
              for (int c = 0; c < 5; ++c) {
                float d = __builtin_ceilf(xmax(fmaf(fmaxf(fmaxf(S[X][c][0], S[X][c][1]), fmaxf(S[X][c][2], S[X][c][3])), SCL, -mref[c])));
                d = move ? (s0 ? d : fmaxf(d, 0.f)) : 0.f;
                l[c] *= ex2(fminf(-d, 0.f)); mref[c] += d;
                const float p0 = ex2(fmaf(S[X][c][0], SCL, -mref[c])), p1 = ex2(fmaf(S[X][c][1], SCL, -mref[c]));
                const float p2 = ex2(fmaf(S[X][c][2], SCL, -mref[c])), p3 = ex2(fmaf(S[X][c][3], SCL, -mref[c]));
                *(LAS u32x2*)(pw + X * 16384 + c * 2048) = u32x2{pk(p0, p1), pk(p2, p3)};
                ps[c] = (p0 + p1) + (p2 + p3);
                *(LAS float*)(L_MX + X * 1280 + (c * 16 + q) * 16 + w * 4) = mref[c];
                asm volatile("" : "+v"(ps[c]), "+v"(l[c]), "+v"(mref[c]));
                SBAR();
              }
              if (move) { u32 inf = 0x7f800000u; asm volatile("" : "+v"(inf)); *(LAS u32*)(flw + X * 16) = inf; }
            }
            thr = PTHR;
          }
        }
        if (n == 34) { l[0] = fadd(l[0], ps[0]); l[1] = fadd(l[1], ps[1]); }
        if (n == 36) { l[2] = fadd(l[2], ps[2]); l[3] = fadd(l[3], ps[3]); }
        if (n == 37) l[4] = fadd(l[4], ps[4]);
        SBAR();
      });
      if constexpr (MM && EXP_NOWAIT) bar();
      else if constexpr (MM) asm volatile("s_waitcnt lgkmcnt(1)\n\ts_barrier" ::: "memory");
      else asm volatile("s_waitcnt vmcnt(0) lgkmcnt(0)\n\ts_barrier" ::: "memory");
    };
    using I0 = std::integral_constant<int, 0>; using I1 = std::integral_constant<int, 1>;
    step(I0{}, I1{}, 0);
    ST(9); STR(41);
#pragma unroll 1
    for (u32 s = 1; s < 15; s += 2) {
      step(I1{}, I1{}, s); step(I0{}, I1{}, s + 1);
    }
    ST(10); STR(42);
    asm volatile("s_nop 7" ::: "memory");
    step(I1{}, I0{}, 15);
    if (__any(*(const LAS u32*)(fla + 16) != 0)) adopt(1, false);
    { const u32 q = fresh_lane() & 15;
#pragma unroll
      for (int qt = 0; qt < 5; ++qt) *(LAS float*)(L_LS + (qt * 16 + q) * 16 + w * 4) = xsum(l[qt]);
    }
    init_ab(L_KC + 1024 * w, 4096);
    readWA(0, 0); readWA(1, 16384); readWA(2, 32768); readWA(3, 49152);
    init_rv(DM * 2);
    pin4(WA[0]); pin4(WA[1]);
#pragma unroll
    for (int i = 0; i < 8; ++i) dma(rWo, rv[i], wo_soff(2), L_KC + 1024 * w + 4096 * i);
    bar_lds();
#pragma unroll
    for (int i = 0; i < 8; ++i) dma(rWo, rv[i], wo_soff(3), L_KC + 32768 + 1024 * w + 4096 * i);
    wait_vm<8>();
    readWA(4, 0); readWA(5, 16384);
    wait_vm<0>();
    readWA(6, 32768); readWA(7, 49152);
    ST(58);
    bar();
    ST(59);
  } else {
    const u32 lane = fresh_lane(), q = lane & 15, g = lane >> 4;
    init_rv(HQ * DH * 2);
    init_ab(slot, 1024);
    zero_acc();
    issue(rWq, wq_soff(1), slot + 8192);
    const rsrc_t rV = mkrsrc(vc + (size_t)(b * HKV + kvh) * SK * DH, KV_BYTES);
    const u32 wa = ((g >> 1) * 16 + q) * 16 + (g & 1) * 8;
    wait_vm<12>();
    ST(34);
    readA(A0, ab, 0);
    {
      u32 xb = L_X + 5 * 10240 + (q >> 3) * 1024 + (q & 7) * 128 + 16 * (g ^ (q & 6));
      bar();
      ST(1);
      readB(B0, xb);
      int dx = -5 * 10240;
#pragma unroll 1
      for (u32 vp = 0; vp < 5; ++vp) {
        gstep<true, false, 9, 9>(acc, A0, B0, B1, ab, slot, xb ^ 64, rWq, wq_soff(vp + 2), rv,
                                 [&]() AI { pin5(B1); bar();   xb += dx; dx = 10240; return xb; }, NoEp{});
      }
      bar();
      ST(2);
      gstep<false, false, 8, 4>(acc, A0, B0, B1, ab, slot, xb ^ 64, rV, 0, rv, [&]() AI { xb += 10240; return xb; }, NoEp{});
      ST(3);
      u32 qa = slot + wa, qa2 = L_P + 2048 * w + wa;
      asm volatile("" : "+v"(qa), "+v"(qa2));
      u32x2 ew;
      gstep<false, true, 0, 0>(acc, A0, B0, B1, ab, slot, xb ^ 64, rWq, 0, rv, [&]() AI { return 0u; }, [&](auto sc, auto ic) AI {
        constexpr int st = sc.value, dt = ic.value % 4, qt = ic.value / 4;
        if constexpr (st == 1) ew = u32x2{pk(acc[dt][qt][0], acc[dt][qt][1]), pk(acc[dt][qt][2], acc[dt][qt][3])};
        if constexpr (st == 2) *(LAS u32x2*)((qt < 4 ? qa + qt * 2048 : qa2) + (dt >> 1) * 1024 + (dt & 1) * 512) = ew;
      });
      ST(4);
      bar_lds();
      init_rv(512);
      issue(rV, tile_off(0) + w * 128, slot);
      zero_acc();
      bar();
      issue(rV, tile_off(1) + w * 128, slot ^ 8192u);
      ST(5);
    }
    float mref[5];
#pragma unroll
    for (int qt = 0; qt < 5; ++qt) mref[qt] = 0.f;
    u32 pb = L_P + lane * 16;
    u32 fla = L_FLAG + (lane & 3) * 4;
    bar();
    ST(6);
    u32 fl = *(const LAS u32*)fla;
    readB(B0, pb);
    wait_vm<12>();
    readA(A0, ab, 0);
    rsrc_t rr = rV;
    float f1[5];
    bool rare;
    auto pre = [&](u32 s) AI {
      rare = __any(fl != 0);
      if (__builtin_expect(rare, 0)) {
        const u32x4 f4 = ldu4(L_FLAG + (s & 1) * 16);
#pragma unroll
        for (int qt = 0; qt < 5; ++qt) {
          const f32x4 m4 = ldf4(L_MX + (s & 1) * 1280 + (qt * 16 + q) * 16);
          float r[4];
#pragma unroll
          for (int i = 0; i < 4; ++i) r[i] = f4[i] ? m4[i] : mref[qt];
          const float M = fmaxf(fmaxf(r[0], r[1]), fmaxf(r[2], r[3]));
          const float f = ex2(fminf(mref[qt] - M, 0.f));
#pragma unroll
          for (int dt = 0; dt < 4; ++dt) acc[dt][qt] *= f;
          B0[qt] = scale8(B0[qt], ex2(((g >> 1) ? r[1] : r[0]) - M));
          f1[qt] = ex2(((g >> 1) ? r[3] : r[2]) - M);
          mref[qt] = M;
        }
      }
    };
    auto mid = [&]() AI {
      pb ^= 16384u; fla ^= 16u;
      pin5(B1);
      if (__builtin_expect(rare, 0)) {
#pragma unroll
        for (int qt = 0; qt < 5; ++qt) B1[qt] = scale8(B1[qt], f1[qt]);
      }
      bar();
    };
#pragma unroll 1
    for (u32 s = 0; s < 15; ++s) {
      if (NO_PV) { bar(); continue; }
      u32 rsoff = tile_off((s + 2) & 15) + w * 128;
      if (__builtin_expect(s == 11, 0)) {
        if (w < 2) touch(rWo, (w * 48 + fresh_lane()) * 128, grp * 16 * (DM * 2));
      }
      if (__builtin_expect(s == 14, 0)) {
        rr = rWo; rsoff = wo_soff(0); init_rv(DM * 2);
        if (w == 3) { rr[2] = 0; slot |= 0x30000u; }
      }
      pre(s);
      gstep<true, false, 9, 9, (KV_NT & 2) != 0>(acc, A0, B0, B1, ab, slot, pb + 1024, rr, rsoff, rv, [&]() AI { mid(); fl = *(const LAS u32*)fla; return pb; }, NoEp{});
    }
    ST(56);
    pre(15);
    float inv[5];
    u32 oa = (w < 3 ? L_O + w * 10240 : L_O3) + wa;
    asm volatile("" : "+v"(oa));
    u32x2 ew;
    gstep<true, true, 9, 0>(acc, A0, B0, B1, ab, slot, pb + 1024, rr, wo_soff(1), rv, [&]() AI {
      mid();
#pragma unroll
      for (int qt = 0; qt < 5; ++qt) { const f32x4 l4 = ldf4(L_LS + (qt * 16 + q) * 16); inv[qt] = __builtin_amdgcn_rcpf((l4[0] + l4[1]) + (l4[2] + l4[3])); }
      return 0u;
    }, [&](auto sc, auto ic) AI {
      constexpr int st = sc.value, dt = ic.value % 4, qt = ic.value / 4;
      if constexpr (st == 0) { acc[dt][qt] *= inv[qt]; asm volatile("" : "+v"(acc[dt][qt])); }
      if constexpr (st == 1) ew = u32x2{pk(acc[dt][qt][0], acc[dt][qt][1]), pk(acc[dt][qt][2], acc[dt][qt][3])};
      if constexpr (st == 2) *(LAS u32x2*)(oa + qt * 2048 + (dt >> 1) * 1024 + (dt & 1) * 512) = ew;
    });
    ST(58);
    bar_lds();
    ST(59);
    if (w < 3) {
      const u32 sl = L_VF + 16384 * w;
      init_ab(sl, 1024);
      wait_vm<0>();
      readWA(0, 8192); readWA(1, 12288); readWA(2, 0); readWA(3, 4096);
      pin4(WA[0]); pin4(WA[1]);
      issue(rWo, wo_soff(2), sl + 8192);
      pin4(WA[2]); pin4(WA[3]);
      issue(rWo, wo_soff(3), sl);
      wait_vm<8>();
      readWA(4, 8192); readWA(5, 12288);
      wait_vm<0>();
      readWA(6, 0); readWA(7, 4096);
    }
  }
  {
    const u32 lane = fresh_lane(), stage = wid >> 2;
    gu64* flags = (gu64*)(ws + W_FLAG + grp * 2 * HQ * 128);
    const unsigned long long tag = *(const LAS unsigned long long*)(L_SYNC + 16);
    if (wid != 7) {
      const __amdgpu_buffer_rsrc_t rP = __builtin_amdgcn_make_buffer_rsrc(ws + W_PART + (size_t)(grp * HQ + head) * PART_BYTES, (short)0, PART_BYTES, 0x00020000);
      u32 ob = L_O + lane * 16, ob3 = L_O3 + lane * 16;
      asm volatile("" : "+v"(ob), "+v"(ob3));
      const u32 po = wid * 10240 + lane * 16;
      bf16x8 OB[2][8];
      f32x4 oc[2][4];
      u32x4 pv;
      auto rdO = [&](int qt, int k) AI { OB[qt & 1][k] = ld128((k < 6 ? ob + (k >> 1) * 10240 : ob3) + qt * 2048 + (k & 1) * 1024); };
#pragma unroll
      for (int k = 0; k < 8; ++k) rdO(0, k);
      sfor<172>([&](auto nc) AI {
        constexpr int n = nc.value, qt = n / 32, k = (n % 32) / 4, dt = n % 4, m = n % 32, X = qt & 1;
        if constexpr (n < 160) {
          oc[X][dt] = mfma(WA[k][dt], OB[X][k], k == 0 ? f32x4{0.f, 0.f, 0.f, 0.f} : oc[X][dt]);
          SBAR();
          if constexpr (qt < 4 && dt == 1) rdO(qt + 1, k);
        }
        if constexpr (qt >= 1) {
          constexpr int np = m >= 6 ? 1 : 0, j = m - 2 - 4 * np;
          if constexpr (m >= 2 && m < 10 && j == 0) { pv[0] = pk(oc[X ^ 1][2 * np][0], oc[X ^ 1][2 * np][1]); pv[1] = pk(oc[X ^ 1][2 * np][2], oc[X ^ 1][2 * np][3]); }
          if constexpr (m >= 2 && m < 10 && j == 1) { pv[2] = pk(oc[X ^ 1][2 * np + 1][0], oc[X ^ 1][2 * np + 1][1]); pv[3] = pk(oc[X ^ 1][2 * np + 1][2], oc[X ^ 1][2 * np + 1][3]); }
          if constexpr (m >= 2 && m < 10 && j == 2) __builtin_amdgcn_raw_buffer_store_b128(pv, rP, po, ((qt - 1) * 2 + np) * 1024, 16);
        }
        SBAR();
      });
      ST(61);
      wait_vm<0>();
      ST(62); STR(44);
      if (lane == 0) {
        const u32 n = __hip_atomic_fetch_add((LAS u32*)(L_SYNC + stage * 4), 1u, __ATOMIC_RELAXED, __HIP_MEMORY_SCOPE_WORKGROUP);
        if (n == 3 - stage) __hip_atomic_store(flags + (stage * HQ + head) * 16, tag, __ATOMIC_RELAXED, __HIP_MEMORY_SCOPE_AGENT);
      }
      const u32 tid = w * 64 + lane, cnt = stage ? 120 : 160;
      if (w * 64 < cnt) {
#pragma unroll 1
        for (u32 spins = 0; spins < (1u << 26); ++spins) {
          if (*(volatile LAS u32*)(L_SYNC + 8 + stage * 4) != 0) break;
          __builtin_amdgcn_s_sleep(2);
        }
        asm volatile("" ::: "memory");
        ST(63); STR(45);
        if (tid < cnt) {
          const __amdgpu_buffer_rsrc_t rG = __builtin_amdgcn_make_buffer_rsrc(ws + W_PART + (size_t)grp * HQ * PART_BYTES, (short)0, HQ * PART_BYTES, 0x00020000);
          const u32 t = stage * 2560 + head * cnt + tid, ln = t & 63, unit = t >> 6;
          u32x4 v[16];
#pragma unroll
          for (int h = 0; h < 16; ++h) v[h] = __builtin_amdgcn_raw_buffer_load_b128(rG, t * 16, h * PART_BYTES, 16);
          float a[8] = {0.f, 0.f, 0.f, 0.f, 0.f, 0.f, 0.f, 0.f};
#pragma unroll
          for (int h = 0; h < 16; ++h)
#pragma unroll
            for (int i = 0; i < 4; ++i) { a[2 * i] += __uint_as_float(v[h][i] << 16); a[2 * i + 1] += __uint_as_float(v[h][i] & 0xffff0000u); }
          const u32 ub = unit / 10, r = unit - 10 * ub;
          u16* o = out + (size_t)(grp * ROWS + (r >> 1) * 16 + (ln & 15)) * DM + ub * 64 + (r & 1) * 32 + 4 * (ln >> 4);
          *(u32x2*)o = u32x2{pk(a[0], a[1]), pk(a[2], a[3])};
          *(u32x2*)(o + 16) = u32x2{pk(a[4], a[5]), pk(a[6], a[7])};
        }
      }
    } else {
#pragma unroll 1
      for (u32 st = 0; st < 2; ++st) {
#pragma unroll 1
        for (u32 spins = 0; spins < (1u << 24); ++spins) {
          const unsigned long long f = __hip_atomic_load(flags + (st * HQ + (lane & 15)) * 16, __ATOMIC_RELAXED, __HIP_MEMORY_SCOPE_AGENT);
          if (__all(f == tag)) break;
          __builtin_amdgcn_s_sleep(2);
        }
        *(volatile LAS u32*)(L_SYNC + 8 + st * 4) = 1;
      }
      if (head == 0 && lane == 0) __hip_atomic_store((gu64*)(ws + W_EPOCH + grp * 128), tag, __ATOMIC_RELAXED, __HIP_MEMORY_SCOPE_AGENT);
    }
  }
  ST_OUT();
}

extern "C" void kernel_launch(void* const* d_in, const int* in_sizes, int n_in, void* d_out, int out_size, void* d_ws, size_t ws_size,
                              hipStream_t stream) {
  (void)in_sizes; (void)n_in; (void)out_size; (void)ws_size;
  (void)hipFuncSetAttribute((const void*)fused_attn, hipFuncAttributeMaxDynamicSharedMemorySize, (int)L_END);
  hipLaunchKernelGGL(fused_attn, dim3(256), dim3(512), L_END, stream, (const u16*)d_in[0], (const u16*)d_in[1], (const u16*)d_in[2],
                     (const u16*)d_in[3], (const u16*)d_in[4], (char*)d_ws, (u16*)d_out);
}
```
